```python
import torch

def _softmax(x, axis=-1):
    return torch.softmax(x, dim=axis)
def _gelu(x):
    return torch.nn.functional.gelu(x, approximate="tanh")
def _silu(x):
    return torch.nn.functional.silu(x)
def _sigmoid(x):
    return torch.sigmoid(x)
def _layernorm(x, axis=-1, eps=1e-5):
    m = x.mean(dim=axis, keepdim=True)
    v = x.var(dim=axis, keepdim=True, unbiased=False)
    return (x - m) / torch.sqrt(v + eps)
def _scatter_add(tgt, idx, src, axis=0):
    return tgt.clone().index_add_(axis, idx, src)
def _segment_sum(src, seg, n):
    out = torch.zeros((n,) + src.shape[1:], dtype=src.dtype)
    return out.index_add_(0, seg, src)
def _pad(x, pw):
    flat = tuple(v for pair in reversed(pw) for v in pair)
    return torch.nn.functional.pad(x, flat)

kernel_name = 'parallel_sum__ssm_conv_s5__dense_activation_squared_relu__prompt'

def setup_inputs():

    _w = (lambda a: (lambda t: t.to(torch.bfloat16) if t.is_floating_point() else t)(torch.as_tensor(a)))
    def _A_setup():
        B, S, D = 1, 2048, 512
        P = D
        Lambda_re = -0.5 * (lambda _x: torch.exp(torch.as_tensor(_x)))((lambda *s: torch.randn(*s, dtype=torch.bfloat16) * 0.8)(P))
        Lambda_im = (lambda *s: torch.randn(*s, dtype=torch.bfloat16) * 0.8)(P) * 1.0
        B_re = (lambda *s: torch.randn(*s, dtype=torch.bfloat16) * 1.6)(P, D) * (D ** -0.5)
        B_im = (lambda *s: torch.randn(*s, dtype=torch.bfloat16) * 1.6)(P, D) * (D ** -0.5)
        C_re = (lambda *s: torch.randn(*s, dtype=torch.bfloat16) * 1.6)(D, P) * (P ** -0.5)
        C_im = (lambda *s: torch.randn(*s, dtype=torch.bfloat16) * 1.6)(D, P) * (P ** -0.5)
        log_dt = (lambda *s: torch.randn(*s, dtype=torch.bfloat16) * 0.8)(P) * 0.3 - 2.0
        Dskip = (lambda *s: torch.randn(*s, dtype=torch.bfloat16) * 0.4)(D)
        return {
            "x": _w((lambda *s: torch.randn(*s, dtype=torch.bfloat16) * 0.05)(B, S, D)),
            "Lambda_re": _w(Lambda_re),
            "Lambda_im": _w(Lambda_im),
            "B_re": _w(B_re),
            "B_im": _w(B_im),
            "C_re": _w(C_re),
            "C_im": _w(C_im),
            "log_dt": _w(log_dt),
            "Dskip": _w(Dskip),
        }
    _A_in = _A_setup()

    def _B_setup():
        B, S, D, F = 1, 2048, 512, 1792
        return {
            "x": _w((lambda *s: torch.randn(*s, dtype=torch.bfloat16) * 0.05)(B, S, D)),
            "w1": _w((lambda *s: torch.randn(*s, dtype=torch.bfloat16) * 0.05)(D, F)),
            "b1": _w((lambda *s: torch.randn(*s, dtype=torch.bfloat16) * 0.2)(F)),
            "w2": _w((lambda *s: torch.randn(*s, dtype=torch.bfloat16) * 0.05)(F, D)),
            "b2": _w((lambda *s: torch.randn(*s, dtype=torch.bfloat16) * 1.6)(D)),
        }
    _B_in = _B_setup()

    return {
        "A_x": _A_in["x"],
        "A_Lambda_re": _A_in["Lambda_re"],
        "A_Lambda_im": _A_in["Lambda_im"],
        "A_B_re": _A_in["B_re"],
        "A_B_im": _A_in["B_im"],
        "A_C_re": _A_in["C_re"],
        "A_C_im": _A_in["C_im"],
        "A_log_dt": _A_in["log_dt"],
        "A_Dskip": _A_in["Dskip"],
        "B_x": _B_in["x"],
        "B_w1": _B_in["w1"],
        "B_b1": _B_in["b1"],
        "B_w2": _B_in["w2"],
        "B_b2": _B_in["b2"],
    }

def reference(A_x, A_Lambda_re, A_Lambda_im, A_B_re, A_B_im, A_C_re, A_C_im, A_log_dt, A_Dskip, B_x, B_w1, B_b1, B_w2, B_b2):
    def _A_ref(x, Lambda_re, Lambda_im, B_re, B_im, C_re, C_im, log_dt, Dskip):
        Bsz, L, H = x.shape
        Lam = Lambda_re + 1j * Lambda_im
        Bmat = B_re + 1j * B_im
        Cmat = C_re + 1j * C_im
        dt = torch.exp(log_dt)
        Lam_bar = torch.exp(dt * Lam)
        B_bar = ((Lam_bar - 1.0) / Lam)[:, None] * Bmat
        Bu = torch.einsum("bld,pd->blp", x + 0j, B_bar)
        h = torch.zeros_like(Bu[:, 0, :])
        hs = []
        for t in range(L):
            h = Lam_bar[None, :] * h + Bu[:, t, :]
            hs.append(h)
        hseq = torch.stack(hs, 1)
        y = 2.0 * torch.einsum("blp,dp->bld", hseq, Cmat).real + x * Dskip[None, None, :]
        return y

    def _B_ref(x, w1, b1, w2, b2):
        h = torch.einsum("bsd,df->bsf", x, w1) + b1
        zero = torch.zeros_like(h)
        r = torch.where(h > zero, h, zero)
        h = r * r
        out = torch.einsum("bsf,fd->bsd", h, w2) + b2
        return out

    x = A_x
    xn = _layernorm(x, axis=-1)
    return x + _A_ref(xn, A_Lambda_re, A_Lambda_im, A_B_re, A_B_im, A_C_re, A_C_im, A_log_dt, A_Dskip) + _B_ref(xn, B_w1, B_b1, B_w2, B_b2)
```

```cpp
#include <hip/hip_runtime.h>
#include <hip/hip_bf16.h>
typedef unsigned short u16;
__device__ __forceinline__ float bf2f(u16 v){ return __uint_as_float(((unsigned)v)<<16); }
constexpr int L=2048, D=512, P=512, F=1792;

__global__ void k_ln(const u16* x, float* xn){
  int t=blockIdx.x; __shared__ float s[2][512];
  float v=bf2f(x[t*D+threadIdx.x]);
  s[0][threadIdx.x]=v; __syncthreads();
  for(int o=256;o>0;o>>=1){ if(threadIdx.x<o) s[0][threadIdx.x]+=s[0][threadIdx.x+o]; __syncthreads(); }
  float m=s[0][0]/D; float d=v-m;
  s[1][threadIdx.x]=d*d; __syncthreads();
  for(int o=256;o>0;o>>=1){ if(threadIdx.x<o) s[1][threadIdx.x]+=s[1][threadIdx.x+o]; __syncthreads(); }
  float var=s[1][0]/D;
  xn[t*D+threadIdx.x]=d/sqrtf(var+1e-5f);
}
__global__ void k_g1(const float* xn,const u16* Bre,const u16* Bim,const u16* w1,const u16* b1,float* ure,float* uim,float* hid){
  int t=blockIdx.x; __shared__ float s[512];
  for(int i=threadIdx.x;i<D;i+=blockDim.x) s[i]=xn[t*D+i];
  __syncthreads();
  for(int c=threadIdx.x;c<P;c+=blockDim.x){
    float a=0,b=0;
    for(int d=0;d<D;d++){ a+=s[d]*bf2f(Bre[c*D+d]); b+=s[d]*bf2f(Bim[c*D+d]); }
    ure[t*P+c]=a; uim[t*P+c]=b;
  }
  for(int f=threadIdx.x;f<F;f+=blockDim.x){
    float a=0; for(int d=0;d<D;d++) a+=s[d]*bf2f(w1[d*F+f]);
    a+=bf2f(b1[f]); a=a>0?a:0; hid[t*F+f]=a*a;
  }
}
__global__ void k_scan(const u16* lre,const u16* lim,const u16* ldt,float* ure,float* uim){
  int p=blockIdx.x*blockDim.x+threadIdx.x;
  float lr=bf2f(lre[p]), li=bf2f(lim[p]); float dt=expf(bf2f(ldt[p]));
  float e=expf(dt*lr); float ar=e*cosf(dt*li), ai=e*sinf(dt*li);
  float nr=ar-1.f, ni=ai; float den=lr*lr+li*li;
  float cr=(nr*lr+ni*li)/den, ci=(ni*lr-nr*li)/den;
  float hr=0,hi=0;
  for(int t=0;t<L;t++){
    float xr=ure[t*P+p], xi=uim[t*P+p];
    float br=cr*xr-ci*xi, bi=cr*xi+ci*xr;
    float nr2=ar*hr-ai*hi+br, ni2=ar*hi+ai*hr+bi;
    hr=nr2; hi=ni2; ure[t*P+p]=hr; uim[t*P+p]=hi;
  }
}
__global__ void k_g2(const u16* x,const float* xn,const float* hre,const float* him,const float* hid,const u16* Cre,const u16* Cim,const u16* Dk,const u16* w2,const u16* b2,float* out){
  int t=blockIdx.x; __shared__ float s[2816];
  for(int i=threadIdx.x;i<P;i+=blockDim.x){ s[i]=hre[t*P+i]; s[P+i]=him[t*P+i]; }
  for(int i=threadIdx.x;i<F;i+=blockDim.x) s[2*P+i]=hid[t*F+i];
  __syncthreads();
  for(int d=threadIdx.x;d<D;d+=blockDim.x){
    float a=0; for(int p=0;p<P;p++) a+=s[p]*bf2f(Cre[d*P+p])-s[P+p]*bf2f(Cim[d*P+p]);
    float b=0; for(int f=0;f<F;f++) b+=s[2*P+f]*bf2f(w2[f*D+d]);
    out[t*D+d]=bf2f(x[t*D+d])+2.f*a+xn[t*D+d]*bf2f(Dk[d])+b+bf2f(b2[d]);
  }
}
extern "C" void kernel_launch(void* const* d_in, const int* in_sizes, int n_in,
                              void* d_out, int out_size, void* d_ws, size_t ws_size,
                              hipStream_t stream) {
  const u16* x=(const u16*)d_in[0];
  float* ws=(float*)d_ws;
  float* xn=ws; float* ure=xn+L*D; float* uim=ure+L*P; float* hid=uim+L*P;
  k_ln<<<L,512,0,stream>>>(x,xn);
  k_g1<<<L,256,0,stream>>>(xn,(const u16*)d_in[3],(const u16*)d_in[4],(const u16*)d_in[10],(const u16*)d_in[11],ure,uim,hid);
  k_scan<<<P/64,64,0,stream>>>((const u16*)d_in[1],(const u16*)d_in[2],(const u16*)d_in[7],ure,uim);
  k_g2<<<L,256,0,stream>>>(x,xn,ure,uim,hid,(const u16*)d_in[5],(const u16*)d_in[6],(const u16*)d_in[8],(const u16*)d_in[12],(const u16*)d_in[13],(float*)d_out);
}
```

```cpp
#include <hip/hip_runtime.h>

__global__ __launch_bounds__(256, 1) void s5_mlp_fused(const void* x, const void* bre, const void* bim, const void* w1, void* ws,
    const void* lre, const void* lim, const void* ldt, const void* b1, const void* w2, const void* cre, const void* cim,
    const void* dsk, const void* b2, void* out) {
  __shared__ __attribute__((aligned(16))) char lds[163840];
  asm volatile(
    ".Lstart_%=:\n"
    "s_load_dwordx2 s[4:5], %0, 0\n"
    "s_load_dwordx2 s[12:13], %0, 24\n"
    "s_load_dwordx2 s[16:17], %0, 32\n"
    "v_lshrrev_b32 v5, 6, %2\n"
    "v_and_b32 v1, 63, %2\n"
    "v_readfirstlane_b32 s33, v5\n"
    "v_lshrrev_b32 v2, 4, v1\n"
    "v_lshlrev_b32 v3, 4, v1\n"
    "s_and_b32 s63, s33, 1\n"
    "s_lshl_b32 s70, s63, 3\n"
    "s_add_u32 s70, s70, 8\n"
    "s_load_dwordx2 s[8:9], %0, s70\n"
    "s_add_u32 s71, s70, 72\n"
    "s_load_dwordx2 s[40:41], %0, s71\n"
    "s_load_dwordx4 s[44:47], %0, 40\n"
    "s_load_dwordx2 s[34:35], %0, 56\n"
    "s_load_dwordx2 s[52:53], %0, 64\n"
    "s_load_dwordx2 s[20:21], %0, 72\n"
    "s_load_dwordx2 s[24:25], %0, 80\n"
    "s_load_dwordx2 s[28:29], %0, 88\n"
    "s_load_dwordx4 s[48:51], %0, 96\n"
    "s_load_dwordx2 s[36:37], %0, 112\n"
    "s_getpc_b64 s[54:55]\n"
    ".Lpc_%=:\n"
    "s_lshr_b32 s70, %1, 3\n"
    "s_lshl_b32 s70, s70, 2\n"
    "s_add_u32 s70, s70, s33\n"
    "s_and_b32 s70, s70, 31\n"
    "s_lshl_b32 s70, s70, 10\n"
    "v_add_u32 v5, s70, v3\n"
    "v_min_u32 v5, .Lend_%=-.Lpc_%=-16, v5\n"
    "global_load_dwordx4 a[0:3], v5, s[54:55]\n"
    "s_lshr_b32 s62, s33, 1\n"
    "s_bfe_u32 s70, %1, 0x20001\n"
    "s_lshr_b32 s71, %1, 3\n"
    "s_mul_i32 s65, s70, 28\n"
    "s_add_u32 s65, s65, s71\n"
    "s_and_b32 s64, s65, 7\n"
    "s_lshr_b32 s65, s65, 3\n"
    "s_lshl_b32 s67, s70, 2\n"
    "s_add_u32 s68, s67, s71\n"
    "s_sub_u32 s68, s68, 28\n"
    "s_lshr_b32 s69, s68, 1\n"
    "s_and_b32 s68, s68, 1\n"
    "s_add_u32 s68, s68, 14\n"
    "s_mov_b32 s14, 0x1c0000\n"
    "s_cmp_ge_u32 s71, 28\n"
    "s_cselect_b32 s64, s69, s64\n"
    "s_cselect_b32 s65, s68, s65\n"
    "s_cselect_b32 s14, 0, s14\n"
    "s_and_b32 s69, %1, 1\n"
    "s_lshl_b32 s70, s69, 3\n"
    "s_add_u32 s64, s64, s70\n"
    "s_lshl_b32 s66, s64, 1\n"
    "s_add_u32 s66, s66, s62\n"
    "s_mov_b32 s6, 0x200000\n"
    "s_mov_b32 s7, 0x20000\n"
    "s_mov_b32 s10, 0x80000\n"
    "s_mov_b32 s11, 0x20000\n"
    "s_mov_b32 s15, 0x20000\n"
    "s_mov_b32 s18, 0xf6d000\n"
    "s_mov_b32 s19, 0x20000\n"
    "v_lshrrev_b32 v20, 3, v1\n"
    "v_and_b32 v21, 7, v1\n"
    "v_xor_b32 v21, v21, v2\n"
    "v_lshlrev_b32 v21, 4, v21\n"
    "s_lshl_b32 s70, s66, 6\n"
    "s_lshl_b32 s71, s63, 3\n"
    "s_add_u32 s70, s70, s71\n"
    "v_and_b32 v5, 32, v1\n"
    "v_lshrrev_b32 v5, 1, v5\n"
    "v_and_or_b32 v5, v20, 3, v5\n"
    "v_add_u32 v5, s70, v5\n"
    "v_lshl_or_b32 v10, v5, 10, v21\n"
    "v_xor_b32 v11, 64, v10\n"
    "v_add_u32 v11, 31744, v11\n"
    "v_add_u32 v12, 2048, v10\n"
    "v_add_u32 v13, 2048, v11\n"
    "s_lshl_b32 s70, s65, 5\n"
    "s_lshl_b32 s71, s62, 4\n"
    "s_add_u32 s70, s70, s71\n"
    "v_add_u32 v5, s70, v20\n"
    "v_lshl_or_b32 v14, v5, 10, v21\n"
    "v_xor_b32 v15, 64, v14\n"
    "v_add_u32 v15, 7168, v15\n"
    "v_bfe_u32 v5, v1, 1, 3\n"
    "v_xor_b32 v5, v5, v2\n"
    "v_lshlrev_b32 v5, 5, v5\n"
    "v_and_or_b32 v5, v3, 16, v5\n"
    "v_xor_b32 v6, 0x80, v5\n"
    "s_mul_i32 s70, s33, 57344\n"
    "s_lshl_b32 s71, s65, 8\n"
    "s_add_u32 s70, s70, s71\n"
    "v_mul_u32_u24 v7, 3584, v2\n"
    "v_add_u32 v7, s70, v7\n"
    "v_add_u32 v16, v5, v7\n"
    "v_add_u32 v17, 13312, v16\n"
    "v_add_u32 v18, v6, v7\n"
    "v_add_u32 v19, 39936, v18\n"
    "v_add_u32 v18, 26624, v18\n"
    "s_lshl_b32 s73, s33, 12\n"
    "s_lshl_b32 s74, s33, 11\n"
    "s_add_u32 s74, s74, 16384\n"
    "s_add_u32 s75, s73, 73728\n"
    "s_lshl_b32 s78, s64, 4\n"
    "s_add_u32 s78, s78, s65\n"
    "s_lshl_b32 s78, s78, 2\n"
    "s_add_u32 s78, s78, s33\n"
    "s_lshl_b32 s81, s78, 11\n"
    "v_and_b32 v4, 15, v1\n"
    "s_waitcnt lgkmcnt(0)\n"
    "buffer_load_dwordx2 v[22:23], off, s[16:19], 0 sc1\n"
    "buffer_load_dwordx4 v[24:27], v3, s[4:7], s81 offen\n"
    "buffer_load_dwordx4 v[28:31], v3, s[4:7], s81 offen offset:1024\n"
    "s_movk_i32 s76, 0\n"
    "s_add_u32 m0, s73, 0\n"
    "s_nop 0\n"
    "buffer_load_dwordx4 v10, s[4:7], s76 offen offset:0 lds\n"
    "buffer_load_dwordx4 v11, s[4:7], s76 offen offset:1024 lds\n"
    "buffer_load_dwordx4 v12, s[4:7], s76 offen offset:2048 lds\n"
    "buffer_load_dwordx4 v13, s[4:7], s76 offen offset:3072 lds\n"
    "s_add_u32 m0, s74, 0\n"
    "s_nop 0\n"
    "buffer_load_dwordx4 v14, s[8:11], s76 offen offset:0 lds\n"
    "s_mov_b64 s[84:85], s[12:13]\n"
    "s_mov_b32 s86, 0x1c0000\n"
    "s_mov_b32 s87, 0x20000\n"
    "s_lshl_b32 s80, %1, 2\n"
    "s_add_u32 s80, s80, s33\n"
    "s_mul_i32 s79, s80, 1792\n"
    "s_lshr_b32 s80, s80, 1\n"
    "s_lshl_b32 s80, s80, 10\n"
    "s_lshr_b32 s70, %1, 6\n"
    "s_add_u32 s67, s67, s70\n"
    "s_lshl_b32 s67, s67, 1\n"
    "s_add_u32 s67, s67, s69\n"
    "s_bfe_u32 s68, %1, 0x30003\n"
    "s_lshl_b32 s69, s69, 2\n"
    "s_add_u32 s69, s69, s33\n"
    "s_lshl_b32 s71, s65, 1\n"
    "s_add_u32 s71, s71, s63\n"
    "s_lshl_b32 s70, s71, 5\n"
    "v_lshl_add_u32 v9, v4, 1, s70\n"
    "s_min_u32 s71, s71, 27\n"
    "s_lshl_b32 s71, s71, 7\n"
    "v_lshl_add_u32 v40, v2, 3, s71\n"
    "s_mov_b32 s22, 0x1c0000\n"
    "s_mov_b32 s23, 0x20000\n"
    "s_mov_b32 s26, 0x80000\n"
    "s_mov_b32 s27, 0x20000\n"
    "s_mov_b32 s30, 0x80000\n"
    "s_mov_b32 s31, 0x20000\n"
    "s_mov_b32 s38, 0x400000\n"
    "s_mov_b32 s39, 0x20000\n"
    "s_mov_b32 s42, 0x80000\n"
    "s_mov_b32 s43, 0x20000\n"
    "s_mov_b32 s72, 65536\n"
    "s_mov_b32 s60, 0\n"
    "s_mov_b32 s61, -1\n"
    "v_lshrrev_b32 v58, 1, v4\n"
    "v_xor_b32 v5, v58, v2\n"
    "s_lshl_b32 s70, s62, 2\n"
    "v_xor_b32 v5, s70, v5\n"
    "v_lshlrev_b32 v5, 4, v5\n"
    "v_lshl_or_b32 v45, v4, 7, v5\n"
    "s_lshl_b32 s70, s62, 13\n"
    "v_add_u32 v46, s70, v45\n"
    "v_xor_b32 v47, 64, v46\n"
    "s_lshl_b32 s71, s63, 12\n"
    "s_add_u32 s71, s71, 16384\n"
    "v_add_u32 v48, s71, v45\n"
    "v_xor_b32 v49, 64, v48\n"
    "v_bfe_u32 v5, v1, 2, 3\n"
    "v_bfe_u32 v59, v1, 2, 2\n"
    "v_and_b32 v60, 3, v1\n"
    "v_lshlrev_b32 v60, 3, v60\n"
    "s_lshl_b32 s71, s63, 2\n"
    "v_xor_b32 v5, s71, v5\n"
    "v_lshl_or_b32 v5, v5, 5, v60\n"
    "v_lshl_add_u32 v6, v2, 3, v59\n"
    "v_lshl_or_b32 v5, v6, 8, v5\n"
    "s_add_u32 s71, s70, 73728\n"
    "v_add_u32 v50, s71, v5\n"
    "s_sub_u32 s71, 81920, s70\n"
    "v_add_u32 v54, s71, v5\n"
    "v_xor_b32 v51, 32, v50\n"
    "v_xor_b32 v52, 64, v50\n"
    "v_xor_b32 v53, 96, v50\n"
    "v_xor_b32 v55, 32, v54\n"
    "v_xor_b32 v56, 64, v54\n"
    "v_xor_b32 v57, 96, v54\n"
    "v_mov_b32 v5, 0x3f803f80\n"
    "v_accvgpr_write_b32 a4, v5\n"
    "v_accvgpr_write_b32 a5, v5\n"
    "v_accvgpr_write_b32 a6, v5\n"
    "v_accvgpr_write_b32 a7, v5\n"
    "v_bfe_u32 v5, v1, 1, 2\n"
    "v_xor_b32 v5, v5, v2\n"
    "v_lshlrev_b32 v5, 5, v5\n"
    "v_and_or_b32 v5, v3, 16, v5\n"
    "v_lshl_or_b32 v5, v20, 10, v5\n"
    "s_lshl_b32 s70, s68, 7\n"
    "v_add_u32 v61, s70, v5\n"
    "v_add_u32 v62, 7168, v61\n"
    "v_add_u32 v63, 14336, v61\n"
    "v_add_u32 v64, 21504, v61\n"
    "s_lshl_b32 s70, s68, 16\n"
    "s_lshl_b32 s71, s69, 7\n"
    "s_add_u32 s70, s70, s71\n"
    "v_lshl_or_b32 v5, v20, 10, v21\n"
    "v_add_u32 v65, s70, v5\n"
    "v_xor_b32 v66, 64, v65\n"
    "v_add_u32 v66, 7168, v66\n"
    "v_add_u32 v67, 14336, v65\n"
    "v_add_u32 v68, 14336, v66\n"
    "s_mul_i32 s83, s69, 229376\n"
    "s_lshr_b32 s70, s67, 1\n"
    "s_lshl_b32 s70, s70, 9\n"
    "s_mul_i32 s88, s69, 7\n"
    "s_lshr_b32 s88, s88, 1\n"
    "s_lshl3_add_u32 s88, s88, s70\n"
    "s_add_u32 s88, s88, 36864\n"
    "s_lshl_b32 s89, s69, 5\n"
    "s_add_u32 s89, s89, s70\n"
    "s_add_u32 s89, s89, 45056\n"
    "v_bfe_u32 v69, v1, 2, 1\n"
    "v_lshl_or_b32 v69, v69, 8, v60\n"
    "s_waitcnt vmcnt(5)\n"
    "v_readfirstlane_b32 s56, v22\n"
    "v_readfirstlane_b32 s57, v23\n"
    "v_lshlrev_b32 v70, 16, v24\n"
    "v_and_b32 v71, 0xffff0000, v24\n"
    "v_lshlrev_b32 v78, 16, v28\n"
    "v_and_b32 v79, 0xffff0000, v28\n"
    "v_lshlrev_b32 v72, 16, v25\n"
    "v_and_b32 v73, 0xffff0000, v25\n"
    "v_lshlrev_b32 v80, 16, v29\n"
    "v_and_b32 v81, 0xffff0000, v29\n"
    "v_lshlrev_b32 v74, 16, v26\n"
    "v_and_b32 v75, 0xffff0000, v26\n"
    "v_lshlrev_b32 v82, 16, v30\n"
    "v_and_b32 v83, 0xffff0000, v30\n"
    "v_lshlrev_b32 v76, 16, v27\n"
    "v_and_b32 v77, 0xffff0000, v27\n"
    "v_lshlrev_b32 v84, 16, v31\n"
    "v_and_b32 v85, 0xffff0000, v31\n"
    "buffer_load_dwordx4 v15, s[8:11], s76 offen offset:1024 lds\n"
    "s_add_u32 s56, s56, 1\n"
    "v_pk_add_f32 v[86:87], v[70:71], v[72:73]\n"
    "v_pk_add_f32 v[88:89], v[78:79], v[80:81]\n"
    "v_pk_add_f32 v[86:87], v[86:87], v[74:75]\n"
    "v_pk_add_f32 v[88:89], v[88:89], v[82:83]\n"
    "v_pk_add_f32 v[86:87], v[86:87], v[76:77]\n"
    "v_pk_add_f32 v[88:89], v[88:89], v[84:85]\n"
    "v_add_f32 v86, v86, v87\n"
    "v_add_f32 v88, v88, v89\n"
    "s_nop 0\n"
    "v_add_f32_dpp v86, v86, v86 row_shr:1 row_mask:0xf bank_mask:0xf bound_ctrl:1\n"
    "v_add_f32_dpp v88, v88, v88 row_shr:1 row_mask:0xf bank_mask:0xf bound_ctrl:1\n"
    "s_nop 0\n"
    "v_add_f32_dpp v86, v86, v86 row_shr:2 row_mask:0xf bank_mask:0xf bound_ctrl:1\n"
    "v_add_f32_dpp v88, v88, v88 row_shr:2 row_mask:0xf bank_mask:0xf bound_ctrl:1\n"
    "s_nop 0\n"
    "v_add_f32_dpp v86, v86, v86 row_shr:4 row_mask:0xf bank_mask:0xf bound_ctrl:1\n"
    "v_add_f32_dpp v88, v88, v88 row_shr:4 row_mask:0xf bank_mask:0xf bound_ctrl:1\n"
    "s_nop 0\n"
    "v_add_f32_dpp v86, v86, v86 row_shr:8 row_mask:0xf bank_mask:0xf bound_ctrl:1\n"
    "v_add_f32_dpp v88, v88, v88 row_shr:8 row_mask:0xf bank_mask:0xf bound_ctrl:1\n"
    "s_nop 0\n"
    "v_add_f32_dpp v86, v86, v86 row_bcast:15 row_mask:0xa bank_mask:0xf\n"
    "s_mov_b32 s77, 0\n"
    "s_add_u32 m0, s75, 0\n"
    "s_nop 0\n"
    "buffer_load_dwordx4 v16, s[12:15], s77 offen offset:0 lds\n"
    "v_add_f32_dpp v88, v88, v88 row_bcast:15 row_mask:0xa bank_mask:0xf\n"
    "v_add_f32_dpp v86, v86, v86 row_bcast:31 row_mask:0xc bank_mask:0xf\n"
    "s_nop 0\n"
    "v_add_f32_dpp v88, v88, v88 row_bcast:31 row_mask:0xc bank_mask:0xf\n"
    "v_mul_f32 v86, 0x3b000000, v86\n"
    "v_mul_f32 v88, 0x3b000000, v88\n"
    "v_readlane_b32 s90, v86, 63\n"
    "v_readlane_b32 s92, v88, 63\n"
    "v_mov_b32 v23, 0x3727c5ac\n"
    "s_lshl_b32 s78, s78, 5\n"
    "s_add_u32 s78, s78, 4096\n"
    "v_pk_add_f32 v[70:71], v[70:71], s[90:91] op_sel_hi:[1,0] neg_lo:[0,1] neg_hi:[0,1]\n"
    "v_pk_add_f32 v[78:79], v[78:79], s[92:93] op_sel_hi:[1,0] neg_lo:[0,1] neg_hi:[0,1]\n"
    "v_pk_add_f32 v[72:73], v[72:73], s[90:91] op_sel_hi:[1,0] neg_lo:[0,1] neg_hi:[0,1]\n"
    "v_pk_add_f32 v[80:81], v[80:81], s[92:93] op_sel_hi:[1,0] neg_lo:[0,1] neg_hi:[0,1]\n"
    "v_pk_add_f32 v[74:75], v[74:75], s[90:91] op_sel_hi:[1,0] neg_lo:[0,1] neg_hi:[0,1]\n"
    "v_pk_add_f32 v[82:83], v[82:83], s[92:93] op_sel_hi:[1,0] neg_lo:[0,1] neg_hi:[0,1]\n"
    "v_pk_add_f32 v[76:77], v[76:77], s[90:91] op_sel_hi:[1,0] neg_lo:[0,1] neg_hi:[0,1]\n"
    "v_pk_add_f32 v[84:85], v[84:85], s[92:93] op_sel_hi:[1,0] neg_lo:[0,1] neg_hi:[0,1]\n"
    "buffer_load_dwordx4 v17, s[12:15], s77 offen offset:1024 lds\n"
    "v_pk_mul_f32 v[86:87], v[70:71], v[70:71]\n"
    "v_pk_mul_f32 v[88:89], v[78:79], v[78:79]\n"
    "v_pk_fma_f32 v[86:87], v[72:73], v[72:73], v[86:87]\n"
    "v_pk_fma_f32 v[88:89], v[80:81], v[80:81], v[88:89]\n"
    "v_pk_fma_f32 v[86:87], v[74:75], v[74:75], v[86:87]\n"
    "v_pk_fma_f32 v[88:89], v[82:83], v[82:83], v[88:89]\n"
    "v_pk_fma_f32 v[86:87], v[76:77], v[76:77], v[86:87]\n"
    "v_pk_fma_f32 v[88:89], v[84:85], v[84:85], v[88:89]\n"
    "v_add_f32 v86, v86, v87\n"
    "v_add_f32 v88, v88, v89\n"
    "s_nop 0\n"
    "v_add_f32_dpp v86, v86, v86 row_shr:1 row_mask:0xf bank_mask:0xf bound_ctrl:1\n"
    "v_add_f32_dpp v88, v88, v88 row_shr:1 row_mask:0xf bank_mask:0xf bound_ctrl:1\n"
    "s_nop 0\n"
    "v_add_f32_dpp v86, v86, v86 row_shr:2 row_mask:0xf bank_mask:0xf bound_ctrl:1\n"
    "v_add_f32_dpp v88, v88, v88 row_shr:2 row_mask:0xf bank_mask:0xf bound_ctrl:1\n"
    "s_nop 0\n"
    "v_add_f32_dpp v86, v86, v86 row_shr:4 row_mask:0xf bank_mask:0xf bound_ctrl:1\n"
    "v_add_f32_dpp v88, v88, v88 row_shr:4 row_mask:0xf bank_mask:0xf bound_ctrl:1\n"
    "s_nop 0\n"
    "v_add_f32_dpp v86, v86, v86 row_shr:8 row_mask:0xf bank_mask:0xf bound_ctrl:1\n"
    "v_add_f32_dpp v88, v88, v88 row_shr:8 row_mask:0xf bank_mask:0xf bound_ctrl:1\n"
    "buffer_load_dwordx4 v18, s[12:15], s77 offen offset:2048 lds\n"
    "v_add_f32_dpp v86, v86, v86 row_bcast:15 row_mask:0xa bank_mask:0xf\n"
    "v_add_f32_dpp v88, v88, v88 row_bcast:15 row_mask:0xa bank_mask:0xf\n"
    "s_nop 0\n"
    "v_add_f32_dpp v86, v86, v86 row_bcast:31 row_mask:0xc bank_mask:0xf\n"
    "v_add_f32_dpp v88, v88, v88 row_bcast:31 row_mask:0xc bank_mask:0xf\n"
    "v_fmamk_f32 v86, v86, 0x3b000000, v23\n"
    "v_fmamk_f32 v88, v88, 0x3b000000, v23\n"
    "v_rsq_f32 v24, v86\n"
    "v_rsq_f32 v28, v88\n"
    "v_mov_b32 v26, s56\n"
    "v_mov_b32 v27, s57\n"
    "v_mov_b32 v30, s56\n"
    "v_mov_b32 v31, s57\n"
    "v_mul_f32 v25, s90, v24\n"
    "v_mul_f32 v29, s92, v28\n"
    "buffer_load_dwordx4 v19, s[12:15], s77 offen offset:3072 lds\n"
    "s_mov_b32 exec_lo, 0\n"
    "s_brev_b32 exec_hi, 1\n"
    "buffer_store_dwordx4 v[24:27], off, s[16:19], s78 offset:0 sc1\n"
    "buffer_store_dwordx4 v[28:31], off, s[16:19], s78 offset:16 sc1\n"
    "s_mov_b64 exec, -1\n"
    "buffer_load_dwordx4 a[0:3], v3, s[84:87], s79 offen offset:0\n"
    "buffer_load_dwordx4 a[0:3], v3, s[84:87], s79 offen offset:768\n"
    "buffer_load_dwordx4 a[0:3], v3, s[8:11], s80 offen offset:0\n"
    "s_movk_i32 s76, 128\n"
    "s_add_u32 m0, s73, 24576\n"
    "s_nop 0\n"
    "buffer_load_dwordx4 v10, s[4:7], s76 offen offset:0 lds\n"
    "buffer_load_dwordx4 v11, s[4:7], s76 offen offset:1024 lds\n"
    "buffer_load_dwordx4 v12, s[4:7], s76 offen offset:2048 lds\n"
    "buffer_load_dwordx4 v13, s[4:7], s76 offen offset:3072 lds\n"
    "s_add_u32 m0, s74, 24576\n"
    "s_nop 0\n"
    "buffer_load_dwordx4 v14, s[8:11], s76 offen offset:0 lds\n"
    "buffer_load_dwordx4 v15, s[8:11], s76 offen offset:1024 lds\n"
    "s_mov_b32 s77, 229376\n"
    "s_add_u32 m0, s75, 16384\n"
    "s_nop 0\n"
    "buffer_load_dwordx4 v16, s[12:15], s77 offen offset:0 lds\n"
    "s_waitcnt vmcnt(12)\n"
    "s_barrier\n"
    "ds_read_b128 a[8:11], v46 offset:0\n"
    "ds_read_b128 a[40:43], v48 offset:0\n"
    "ds_read_b128 a[44:47], v48 offset:2048\n"
    "ds_read_b128 a[12:15], v46 offset:2048\n"
    "ds_read_b128 a[16:19], v46 offset:4096\n"
    "ds_read_b128 a[20:23], v46 offset:6144\n"
    "ds_read_b64_tr_b16 a[56:57], v50 offset:0\n"
    "ds_read_b64_tr_b16 a[58:59], v50 offset:1024\n"
    "ds_read_b64_tr_b16 a[60:61], v51 offset:0\n"
    "ds_read_b64_tr_b16 a[62:63], v51 offset:1024\n"
    "ds_read_b64_tr_b16 a[64:65], v52 offset:0\n"
    "ds_read_b64_tr_b16 a[66:67], v52 offset:1024\n"
    "ds_read_b64_tr_b16 a[68:69], v53 offset:0\n"
    "ds_read_b64_tr_b16 a[70:71], v53 offset:1024\n"
    "s_waitcnt lgkmcnt(8)\n"
    "v_mfma_f32_16x16x32_bf16 v[28:31], a[8:11], a[40:43], 0\n"
    "s_lshl_b32 s70, s33, 9\n"
    "ds_read_b128 a[24:27], v47 offset:0\n"
    "s_add_u32 s70, s70, 155648\n"
    "v_mfma_f32_16x16x32_bf16 v[96:99], a[8:11], a[44:47], 0\n"
    "buffer_load_dwordx4 v17, s[12:15], s77 offen offset:1024 lds\n"
    "v_mfma_f32_16x16x32_bf16 v[72:75], a[12:15], a[40:43], 0\n"
    "ds_read_b128 a[48:51], v49 offset:0\n"
    "v_lshl_add_u32 v22, v1, 3, s70\n"
    "v_mfma_f32_16x16x32_bf16 v[100:103], a[12:15], a[44:47], 0\n"
    "buffer_load_dwordx4 v18, s[12:15], s77 offen offset:2048 lds\n"
    "v_mfma_f32_16x16x32_bf16 v[88:91], a[16:19], a[40:43], 0\n"
    "ds_read_b128 a[52:55], v49 offset:2048\n"
    "v_lshl_add_u32 v23, v2, 7, s70\n"
    "v_mfma_f32_16x16x32_bf16 v[104:107], a[16:19], a[44:47], 0\n"
    "buffer_load_dwordx4 v19, s[12:15], s77 offen offset:3072 lds\n"
    "v_mfma_f32_16x16x32_bf16 v[92:95], a[20:23], a[40:43], 0\n"
    "s_movk_i32 s76, 256\n"
    "ds_read_b128 a[28:31], v47 offset:2048\n"
    "s_add_u32 m0, s73, 49152\n"
    "v_mfma_f32_16x16x32_bf16 v[108:111], a[20:23], a[44:47], 0\n"
    "buffer_load_dwordx4 v10, s[4:7], s76 offen offset:0 lds\n"
    "v_mfma_f32_16x16x32_bf16 a[104:107], a[4:7], a[40:43], 0\n"
    "ds_read_b128 a[32:35], v47 offset:4096\n"
    "v_lshrrev_b32 v5, 2, v4\n"
    "v_mfma_f32_16x16x32_bf16 a[108:111], a[4:7], a[44:47], 0\n"
    "s_waitcnt lgkmcnt(9)\n"
    "buffer_load_dwordx4 v11, s[4:7], s76 offen offset:1024 lds\n"
    "v_mfma_f32_16x16x32_bf16 a[88:91], a[4:7], a[56:59], 0\n"
    "ds_read_b128 a[36:39], v47 offset:6144\n"
    "v_lshlrev_b32 v5, 4, v5\n"
    "v_mfma_f32_16x16x32_bf16 a[92:95], a[4:7], a[60:63], 0\n"
    "buffer_load_dwordx4 v12, s[4:7], s76 offen offset:2048 lds\n"
    "v_mfma_f32_16x16x32_bf16 v[112:115], a[56:59], a[8:11], 0\n"
    "ds_read_b64_tr_b16 a[72:73], v54 offset:0\n"
    "v_and_or_b32 v5, v4, 3, v5\n"
    "v_mfma_f32_16x16x32_bf16 v[128:131], a[60:63], a[8:11], 0\n"
    "buffer_load_dwordx4 v13, s[4:7], s76 offen offset:3072 lds\n"
    "v_mfma_f32_16x16x32_bf16 v[116:119], a[56:59], a[12:15], 0\n"
    "s_add_u32 m0, s74, 49152\n"
    "ds_read_b64_tr_b16 a[74:75], v54 offset:1024\n"
    "v_lshl_add_u32 v70, v5, 3, s70\n"
    "v_mfma_f32_16x16x32_bf16 v[132:135], a[60:63], a[12:15], 0\n"
    "s_lshl_b32 s70, s63, 9\n"
    "ds_read_b64_tr_b16 a[76:77], v55 offset:0\n"
    "s_add_u32 s70, s70, 157696\n"
    "v_mfma_f32_16x16x32_bf16 v[120:123], a[56:59], a[16:19], 0\n"
    "buffer_load_dwordx4 v14, s[8:11], s76 offen offset:0 lds\n"
    "v_mfma_f32_16x16x32_bf16 v[136:139], a[60:63], a[16:19], 0\n"
    "ds_read_b64_tr_b16 a[78:79], v55 offset:1024\n"
    "v_lshl_add_u32 v76, v2, 4, s70\n"
    "v_mfma_f32_16x16x32_bf16 v[124:127], a[56:59], a[20:23], 0\n"
    "buffer_load_dwordx4 v15, s[8:11], s76 offen offset:1024 lds\n"
    "v_mfma_f32_16x16x32_bf16 v[140:143], a[60:63], a[20:23], 0\n"
    "s_waitcnt lgkmcnt(10)\n"
    "s_mov_b32 s77, 458752\n"
    "ds_read_b64_tr_b16 a[80:81], v56 offset:0\n"
    "s_add_u32 m0, s75, 32768\n"
    "v_mfma_f32_16x16x32_bf16 a[96:99], a[4:7], a[64:67], 0\n"
    "buffer_load_dwordx4 v16, s[12:15], s77 offen offset:0 lds\n"
    "v_mfma_f32_16x16x32_bf16 a[100:103], a[4:7], a[68:71], 0\n"
    "ds_read_b64_tr_b16 a[82:83], v56 offset:1024\n"
    "v_lshl_add_u32 v77, v4, 2, s70\n"
    "v_mfma_f32_16x16x32_bf16 v[144:147], a[64:67], a[8:11], 0\n"
    "buffer_load_dwordx4 v17, s[12:15], s77 offen offset:1024 lds\n"
    "v_mfma_f32_16x16x32_bf16 v[160:163], a[68:71], a[8:11], 0\n"
    "s_lshl_b32 s70, s62, 10\n"
    "ds_read_b64_tr_b16 a[84:85], v57 offset:0\n"
    "v_lshl_add_u32 v71, v2, 18, v77\n"
    "v_mfma_f32_16x16x32_bf16 v[148:151], a[64:67], a[12:15], 0\n"
    "buffer_load_dwordx4 v18, s[12:15], s77 offen offset:2048 lds\n"
    "v_mfma_f32_16x16x32_bf16 v[164:167], a[68:71], a[12:15], 0\n"
    "ds_read_b64_tr_b16 a[86:87], v57 offset:1024\n"
    "v_add_u32 v71, s70, v71\n"
    "v_mfma_f32_16x16x32_bf16 v[152:155], a[64:67], a[16:19], 0\n"
    "buffer_load_dwordx4 v19, s[12:15], s77 offen offset:3072 lds\n"
    "v_mfma_f32_16x16x32_bf16 v[168:171], a[68:71], a[16:19], 0\n"
    "v_mfma_f32_16x16x32_bf16 v[156:159], a[64:67], a[20:23], 0\n"
    "v_mfma_f32_16x16x32_bf16 v[172:175], a[68:71], a[20:23], 0\n"
    "s_waitcnt vmcnt(10) lgkmcnt(0)\n"
    "s_barrier\n"
    "v_mfma_f32_16x16x32_bf16 v[28:31], a[24:27], a[48:51], v[28:31]\n"
    "s_movk_i32 s76, 384\n"
    "ds_read_b128 a[8:11], v46 offset:24576\n"
    "s_add_u32 m0, s73, 0\n"
    "v_mfma_f32_16x16x32_bf16 v[96:99], a[24:27], a[52:55], v[96:99]\n"
    "buffer_load_dwordx4 v10, s[4:7], s76 offen offset:0 lds\n"
    "v_mfma_f32_16x16x32_bf16 v[72:75], a[28:31], a[48:51], v[72:75]\n"
    "ds_read_b128 a[40:43], v48 offset:24576\n"
    "v_mfma_f32_16x16x32_bf16 v[100:103], a[28:31], a[52:55], v[100:103]\n"
    "global_load_ushort v42, v9, s[44:45]\n"
    "v_mfma_f32_16x16x32_bf16 v[88:91], a[32:35], a[48:51], v[88:91]\n"
    "ds_read_b128 a[44:47], v48 offset:26624\n"
    "v_mfma_f32_16x16x32_bf16 v[104:107], a[32:35], a[52:55], v[104:107]\n"
    "ds_read_b128 a[12:15], v46 offset:26624\n"
    "v_mfma_f32_16x16x32_bf16 v[92:95], a[36:39], a[48:51], v[92:95]\n"
    "global_load_ushort v43, v9, s[46:47]\n"
    "v_mfma_f32_16x16x32_bf16 v[108:111], a[36:39], a[52:55], v[108:111]\n"
    "ds_read_b128 a[16:19], v46 offset:28672\n"
    "v_mfma_f32_16x16x32_bf16 v[112:115], a[72:75], a[24:27], v[112:115]\n"
    "ds_read_b128 a[20:23], v46 offset:30720\n"
    "v_mfma_f32_16x16x32_bf16 v[128:131], a[76:79], a[24:27], v[128:131]\n"
    "buffer_load_dwordx4 v11, s[4:7], s76 offen offset:1024 lds\n"
    "v_mfma_f32_16x16x32_bf16 v[116:119], a[72:75], a[28:31], v[116:119]\n"
    "ds_read_b64_tr_b16 a[56:57], v50 offset:16384\n"
    "v_mfma_f32_16x16x32_bf16 v[132:135], a[76:79], a[28:31], v[132:135]\n"
    "global_load_ushort v44, v9, s[34:35]\n"
    "v_mfma_f32_16x16x32_bf16 v[120:123], a[72:75], a[32:35], v[120:123]\n"
    "ds_read_b64_tr_b16 a[58:59], v50 offset:17408\n"
    "v_mfma_f32_16x16x32_bf16 v[136:139], a[76:79], a[32:35], v[136:139]\n"
    "ds_read_b64_tr_b16 a[60:61], v51 offset:16384\n"
    "v_mfma_f32_16x16x32_bf16 v[124:127], a[72:75], a[36:39], v[124:127]\n"
    "global_load_dwordx2 v[32:33], v40, s[52:53] offset:0\n"
    "v_mfma_f32_16x16x32_bf16 v[140:143], a[76:79], a[36:39], v[140:143]\n"
    "ds_read_b64_tr_b16 a[62:63], v51 offset:17408\n"
    "v_mfma_f32_16x16x32_bf16 v[144:147], a[80:83], a[24:27], v[144:147]\n"
    "ds_read_b64_tr_b16 a[64:65], v52 offset:16384\n"
    "v_mfma_f32_16x16x32_bf16 v[160:163], a[84:87], a[24:27], v[160:163]\n"
    "buffer_load_dwordx4 v12, s[4:7], s76 offen offset:2048 lds\n"
    "v_mfma_f32_16x16x32_bf16 v[148:151], a[80:83], a[28:31], v[148:151]\n"
    "ds_read_b64_tr_b16 a[66:67], v52 offset:17408\n"
    "v_mfma_f32_16x16x32_bf16 v[164:167], a[84:87], a[28:31], v[164:167]\n"
    "ds_read_b64_tr_b16 a[68:69], v53 offset:16384\n"
    "v_mfma_f32_16x16x32_bf16 v[152:155], a[80:83], a[32:35], v[152:155]\n"
    "global_load_dwordx2 v[34:35], v40, s[52:53] offset:32\n"
    "v_mfma_f32_16x16x32_bf16 v[168:171], a[84:87], a[32:35], v[168:171]\n"
    "ds_read_b64_tr_b16 a[70:71], v53 offset:17408\n"
    "v_mfma_f32_16x16x32_bf16 v[156:159], a[80:83], a[36:39], v[156:159]\n"
    "global_load_dwordx2 v[36:37], v40, s[52:53] offset:64\n"
    "v_mfma_f32_16x16x32_bf16 v[172:175], a[84:87], a[36:39], v[172:175]\n"
    "s_waitcnt lgkmcnt(8)\n"
    "v_mfma_f32_16x16x32_bf16 v[28:31], a[8:11], a[40:43], v[28:31]\n"
    "ds_read_b128 a[24:27], v47 offset:24576\n"
    "v_mfma_f32_16x16x32_bf16 v[96:99], a[8:11], a[44:47], v[96:99]\n"
    "buffer_load_dwordx4 v13, s[4:7], s76 offen offset:3072 lds\n"
    "v_mfma_f32_16x16x32_bf16 v[72:75], a[12:15], a[40:43], v[72:75]\n"
    "ds_read_b128 a[48:51], v49 offset:24576\n"
    "v_mfma_f32_16x16x32_bf16 v[100:103], a[12:15], a[44:47], v[100:103]\n"
    "ds_read_b128 a[52:55], v49 offset:26624\n"
    "v_mfma_f32_16x16x32_bf16 v[88:91], a[16:19], a[40:43], v[88:91]\n"
    "global_load_dwordx2 v[38:39], v40, s[52:53] offset:96\n"
    "v_mfma_f32_16x16x32_bf16 v[104:107], a[16:19], a[44:47], v[104:107]\n"
    "ds_read_b128 a[28:31], v47 offset:26624\n"
    "v_mfma_f32_16x16x32_bf16 v[92:95], a[20:23], a[40:43], v[92:95]\n"
    "ds_read_b128 a[32:35], v47 offset:28672\n"
    "v_mfma_f32_16x16x32_bf16 v[108:111], a[20:23], a[44:47], v[108:111]\n"
    "buffer_load_dword a0, v69, s[16:19], s88 offen sc1\n"
    "v_mfma_f32_16x16x32_bf16 a[104:107], a[4:7], a[40:43], a[104:107]\n"
    "ds_read_b128 a[36:39], v47 offset:30720\n"
    "v_mfma_f32_16x16x32_bf16 a[108:111], a[4:7], a[44:47], a[108:111]\n"
    "s_waitcnt lgkmcnt(10)\n"
    "buffer_load_dword a0, v69, s[16:19], s89 offen sc1\n"
    "v_mfma_f32_16x16x32_bf16 a[88:91], a[4:7], a[56:59], a[88:91]\n"
    "s_add_u32 m0, s74, 0\n"
    "ds_read_b64_tr_b16 a[72:73], v54 offset:16384\n"
    "v_mfma_f32_16x16x32_bf16 a[92:95], a[4:7], a[60:63], a[92:95]\n"
    "ds_read_b64_tr_b16 a[74:75], v54 offset:17408\n"
    "v_mfma_f32_16x16x32_bf16 v[112:115], a[56:59], a[8:11], v[112:115]\n"
    "buffer_load_dwordx4 v14, s[8:11], s76 offen offset:0 lds\n"
    "v_mfma_f32_16x16x32_bf16 v[128:131], a[60:63], a[8:11], v[128:131]\n"
    "ds_read_b64_tr_b16 a[76:77], v55 offset:16384\n"
    "v_mfma_f32_16x16x32_bf16 v[116:119], a[56:59], a[12:15], v[116:119]\n"
    "ds_read_b64_tr_b16 a[78:79], v55 offset:17408\n"
    "v_mfma_f32_16x16x32_bf16 v[132:135], a[60:63], a[12:15], v[132:135]\n"
    "buffer_load_dwordx4 v15, s[8:11], s76 offen offset:1024 lds\n"
    "v_mfma_f32_16x16x32_bf16 v[120:123], a[56:59], a[16:19], v[120:123]\n"
    "s_mov_b32 s77, 688128\n"
    "ds_read_b64_tr_b16 a[80:81], v56 offset:16384\n"
    "s_add_u32 m0, s75, 0\n"
    "v_mfma_f32_16x16x32_bf16 v[136:139], a[60:63], a[16:19], v[136:139]\n"
    "ds_read_b64_tr_b16 a[82:83], v56 offset:17408\n"
    "v_mfma_f32_16x16x32_bf16 v[124:127], a[56:59], a[20:23], v[124:127]\n"
    "buffer_load_dwordx4 v16, s[12:15], s77 offen offset:0 lds\n"
    "v_mfma_f32_16x16x32_bf16 v[140:143], a[60:63], a[20:23], v[140:143]\n"
    "s_waitcnt lgkmcnt(12)\n"
    "ds_read_b64_tr_b16 a[84:85], v57 offset:16384\n"
    "v_mfma_f32_16x16x32_bf16 a[96:99], a[4:7], a[64:67], a[96:99]\n"
    "buffer_load_dwordx4 v17, s[12:15], s77 offen offset:1024 lds\n"
    "v_mfma_f32_16x16x32_bf16 a[100:103], a[4:7], a[68:71], a[100:103]\n"
    "ds_read_b64_tr_b16 a[86:87], v57 offset:17408\n"
    "v_mfma_f32_16x16x32_bf16 v[144:147], a[64:67], a[8:11], v[144:147]\n"
    "v_mfma_f32_16x16x32_bf16 v[160:163], a[68:71], a[8:11], v[160:163]\n"
    "buffer_load_dwordx4 v18, s[12:15], s77 offen offset:2048 lds\n"
    "v_mfma_f32_16x16x32_bf16 v[148:151], a[64:67], a[12:15], v[148:151]\n"
    "v_mfma_f32_16x16x32_bf16 v[164:167], a[68:71], a[12:15], v[164:167]\n"
    "v_mfma_f32_16x16x32_bf16 v[152:155], a[64:67], a[16:19], v[152:155]\n"
    "buffer_load_dwordx4 v19, s[12:15], s77 offen offset:3072 lds\n"
    "v_mfma_f32_16x16x32_bf16 v[168:171], a[68:71], a[16:19], v[168:171]\n"
    "v_mfma_f32_16x16x32_bf16 v[156:159], a[64:67], a[20:23], v[156:159]\n"
    "v_mfma_f32_16x16x32_bf16 v[172:175], a[68:71], a[20:23], v[172:175]\n"
    "s_waitcnt vmcnt(19) lgkmcnt(0)\n"
    "s_barrier\n"
    "v_mfma_f32_16x16x32_bf16 v[28:31], a[24:27], a[48:51], v[28:31]\n"
    "s_movk_i32 s76, 512\n"
    "ds_read_b128 a[8:11], v46 offset:49152\n"
    "s_add_u32 m0, s73, 24576\n"
    "v_mfma_f32_16x16x32_bf16 v[96:99], a[24:27], a[52:55], v[96:99]\n"
    "buffer_load_dwordx4 v10, s[4:7], s76 offen offset:0 lds\n"
    "v_mfma_f32_16x16x32_bf16 v[72:75], a[28:31], a[48:51], v[72:75]\n"
    "ds_read_b128 a[40:43], v48 offset:49152\n"
    "v_mfma_f32_16x16x32_bf16 v[100:103], a[28:31], a[52:55], v[100:103]\n"
    "ds_read_b128 a[44:47], v48 offset:51200\n"
    "v_mfma_f32_16x16x32_bf16 v[88:91], a[32:35], a[48:51], v[88:91]\n"
    "ds_read_b128 a[12:15], v46 offset:51200\n"
    "v_mfma_f32_16x16x32_bf16 v[104:107], a[32:35], a[52:55], v[104:107]\n"
    "buffer_load_dwordx4 v11, s[4:7], s76 offen offset:1024 lds\n"
    "v_mfma_f32_16x16x32_bf16 v[92:95], a[36:39], a[48:51], v[92:95]\n"
    "ds_read_b128 a[16:19], v46 offset:53248\n"
    "v_mfma_f32_16x16x32_bf16 v[108:111], a[36:39], a[52:55], v[108:111]\n"
    "ds_read_b128 a[20:23], v46 offset:55296\n"
    "v_mfma_f32_16x16x32_bf16 v[112:115], a[72:75], a[24:27], v[112:115]\n"
    "ds_read_b64_tr_b16 a[56:57], v50 offset:32768\n"
    "v_mfma_f32_16x16x32_bf16 v[128:131], a[76:79], a[24:27], v[128:131]\n"
    "buffer_load_dwordx4 v12, s[4:7], s76 offen offset:2048 lds\n"
    "v_mfma_f32_16x16x32_bf16 v[116:119], a[72:75], a[28:31], v[116:119]\n"
    "ds_read_b64_tr_b16 a[58:59], v50 offset:33792\n"
    "v_mfma_f32_16x16x32_bf16 v[132:135], a[76:79], a[28:31], v[132:135]\n"
    "ds_read_b64_tr_b16 a[60:61], v51 offset:32768\n"
    "v_mfma_f32_16x16x32_bf16 v[120:123], a[72:75], a[32:35], v[120:123]\n"
    "ds_read_b64_tr_b16 a[62:63], v51 offset:33792\n"
    "v_mfma_f32_16x16x32_bf16 v[136:139], a[76:79], a[32:35], v[136:139]\n"
    "buffer_load_dwordx4 v13, s[4:7], s76 offen offset:3072 lds\n"
    "v_mfma_f32_16x16x32_bf16 v[124:127], a[72:75], a[36:39], v[124:127]\n"
    "ds_read_b64_tr_b16 a[64:65], v52 offset:32768\n"
    "v_mfma_f32_16x16x32_bf16 v[140:143], a[76:79], a[36:39], v[140:143]\n"
    "ds_read_b64_tr_b16 a[66:67], v52 offset:33792\n"
    "v_mfma_f32_16x16x32_bf16 v[144:147], a[80:83], a[24:27], v[144:147]\n"
    "ds_read_b64_tr_b16 a[68:69], v53 offset:32768\n"
    "v_mfma_f32_16x16x32_bf16 v[160:163], a[84:87], a[24:27], v[160:163]\n"
    "buffer_load_dwordx4 a[0:3], v3, s[20:23], s79 offen offset:0\n"
    "v_mfma_f32_16x16x32_bf16 v[148:151], a[80:83], a[28:31], v[148:151]\n"
    "ds_read_b64_tr_b16 a[70:71], v53 offset:33792\n"
    "v_mfma_f32_16x16x32_bf16 v[164:167], a[84:87], a[28:31], v[164:167]\n"
    "s_add_u32 m0, s74, 24576\n"
    "v_mfma_f32_16x16x32_bf16 v[152:155], a[80:83], a[32:35], v[152:155]\n"
    "v_mfma_f32_16x16x32_bf16 v[168:171], a[84:87], a[32:35], v[168:171]\n"
    "buffer_load_dwordx4 v14, s[8:11], s76 offen offset:0 lds\n"
    "v_mfma_f32_16x16x32_bf16 v[156:159], a[80:83], a[36:39], v[156:159]\n"
    "v_mfma_f32_16x16x32_bf16 v[172:175], a[84:87], a[36:39], v[172:175]\n"
    "s_waitcnt lgkmcnt(8)\n"
    "v_mfma_f32_16x16x32_bf16 v[28:31], a[8:11], a[40:43], v[28:31]\n"
    "s_waitcnt vmcnt(14)\n"
    "ds_read_b128 a[24:27], v47 offset:49152\n"
    "v_lshlrev_b32 v42, 16, v42\n"
    "v_mfma_f32_16x16x32_bf16 v[96:99], a[8:11], a[44:47], v[96:99]\n"
    "buffer_load_dwordx4 v15, s[8:11], s76 offen offset:1024 lds\n"
    "v_mfma_f32_16x16x32_bf16 v[72:75], a[12:15], a[40:43], v[72:75]\n"
    "ds_read_b128 a[48:51], v49 offset:49152\n"
    "v_lshlrev_b32 v43, 16, v43\n"
    "v_mfma_f32_16x16x32_bf16 v[100:103], a[12:15], a[44:47], v[100:103]\n"
    "s_mov_b32 s77, 917504\n"
    "ds_read_b128 a[52:55], v49 offset:51200\n"
    "s_add_u32 m0, s75, 16384\n"
    "v_mfma_f32_16x16x32_bf16 v[88:91], a[16:19], a[40:43], v[88:91]\n"
    "ds_read_b128 a[28:31], v47 offset:51200\n"
    "v_lshlrev_b32 v44, 16, v44\n"
    "v_mfma_f32_16x16x32_bf16 v[104:107], a[16:19], a[44:47], v[104:107]\n"
    "buffer_load_dwordx4 v16, s[12:15], s77 offen offset:0 lds\n"
    "v_mfma_f32_16x16x32_bf16 v[92:95], a[20:23], a[40:43], v[92:95]\n"
    "ds_read_b128 a[32:35], v47 offset:53248\n"
    "v_mul_f32 v44, 0x3fb8aa3b, v44\n"
    "v_mfma_f32_16x16x32_bf16 v[108:111], a[20:23], a[44:47], v[108:111]\n"
    "s_mov_b32 s58, 0xffff0000\n"
    "ds_read_b128 a[36:39], v47 offset:55296\n"
    "v_mfma_f32_16x16x32_bf16 a[104:107], a[4:7], a[40:43], a[104:107]\n"
    "ds_read_b64_tr_b16 a[72:73], v54 offset:32768\n"
    "v_mfma_f32_16x16x32_bf16 a[108:111], a[4:7], a[44:47], a[108:111]\n"
    "s_waitcnt lgkmcnt(11)\n"
    "buffer_load_dwordx4 a[0:3], v3, s[20:23], s79 offen offset:768\n"
    "v_mfma_f32_16x16x32_bf16 a[88:91], a[4:7], a[56:59], a[88:91]\n"
    "ds_read_b64_tr_b16 a[74:75], v54 offset:33792\n"
    "v_mfma_f32_16x16x32_bf16 a[92:95], a[4:7], a[60:63], a[92:95]\n"
    "ds_read_b64_tr_b16 a[76:77], v55 offset:32768\n"
    "v_mfma_f32_16x16x32_bf16 v[112:115], a[56:59], a[8:11], v[112:115]\n"
    "ds_read_b64_tr_b16 a[78:79], v55 offset:33792\n"
    "v_mfma_f32_16x16x32_bf16 v[128:131], a[60:63], a[8:11], v[128:131]\n"
    "buffer_load_dwordx4 v17, s[12:15], s77 offen offset:1024 lds\n"
    "v_mfma_f32_16x16x32_bf16 v[116:119], a[56:59], a[12:15], v[116:119]\n"
    "ds_read_b64_tr_b16 a[80:81], v56 offset:32768\n"
    "v_mfma_f32_16x16x32_bf16 v[132:135], a[60:63], a[12:15], v[132:135]\n"
    "ds_read_b64_tr_b16 a[82:83], v56 offset:33792\n"
    "v_mfma_f32_16x16x32_bf16 v[120:123], a[56:59], a[16:19], v[120:123]\n"
    "ds_read_b64_tr_b16 a[84:85], v57 offset:32768\n"
    "v_mfma_f32_16x16x32_bf16 v[136:139], a[60:63], a[16:19], v[136:139]\n"
    "buffer_load_dwordx4 v18, s[12:15], s77 offen offset:2048 lds\n"
    "v_mfma_f32_16x16x32_bf16 v[124:127], a[56:59], a[20:23], v[124:127]\n"
    "ds_read_b64_tr_b16 a[86:87], v57 offset:33792\n"
    "v_mfma_f32_16x16x32_bf16 v[140:143], a[60:63], a[20:23], v[140:143]\n"
    "s_waitcnt lgkmcnt(14)\n"
    "v_exp_f32 v44, v44\n"
    "v_mfma_f32_16x16x32_bf16 a[96:99], a[4:7], a[64:67], a[96:99]\n"
    "s_mov_b32 s59, 0xffff0000\n"
    "v_mul_f32 v80, v42, v42\n"
    "v_cvt_pk_bf16_f32 v44, v44, v44\n"
    "v_mfma_f32_16x16x32_bf16 a[100:103], a[4:7], a[68:71], a[100:103]\n"
    "buffer_load_dwordx4 v19, s[12:15], s77 offen offset:3072 lds\n"
    "v_mfma_f32_16x16x32_bf16 v[144:147], a[64:67], a[8:11], v[144:147]\n"
    "v_and_b32 v44, 0xffff0000, v44\n"
    "v_mul_f32 v78, v44, v42\n"
    "v_mfma_f32_16x16x32_bf16 v[160:163], a[68:71], a[8:11], v[160:163]\n"
    "v_mul_f32 v79, v44, v43\n"
    "v_mul_f32 v78, 0x3fb8aa3b, v78\n"
    "v_mfma_f32_16x16x32_bf16 v[148:151], a[64:67], a[12:15], v[148:151]\n"
    "v_mul_f32 v79, 0.15915494, v79\n"
    "v_mfma_f32_16x16x32_bf16 v[164:167], a[68:71], a[12:15], v[164:167]\n"
    "buffer_load_dwordx4 a[0:3], v3, s[40:43], s80 offen offset:0\n"
    "v_mfma_f32_16x16x32_bf16 v[152:155], a[64:67], a[16:19], v[152:155]\n"
    "v_exp_f32 v78, v78\n"
    "v_mfma_f32_16x16x32_bf16 v[168:171], a[68:71], a[16:19], v[168:171]\n"
    "v_fract_f32 v79, v79\n"
    "v_fmac_f32 v80, v43, v43\n"
    "v_mfma_f32_16x16x32_bf16 v[156:159], a[64:67], a[20:23], v[156:159]\n"
    "v_cos_f32 v86, v79\n"
    "v_mfma_f32_16x16x32_bf16 v[172:175], a[68:71], a[20:23], v[172:175]\n"
    "v_sin_f32 v87, v79\n"
    "s_waitcnt vmcnt(13) lgkmcnt(0)\n"
    "s_barrier\n"
    "v_mfma_f32_16x16x32_bf16 v[28:31], a[24:27], a[48:51], v[28:31]\n"
    "s_movk_i32 s76, 640\n"
    "ds_read_b128 a[8:11], v46 offset:0\n"
    "s_add_u32 m0, s73, 49152\n"
    "v_mfma_f32_16x16x32_bf16 v[96:99], a[24:27], a[52:55], v[96:99]\n"
    "buffer_load_dwordx4 v10, s[4:7], s76 offen offset:0 lds\n"
    "v_mfma_f32_16x16x32_bf16 v[72:75], a[28:31], a[48:51], v[72:75]\n"
    "ds_read_b128 a[40:43], v48 offset:0\n"
    "v_mfma_f32_16x16x32_bf16 v[100:103], a[28:31], a[52:55], v[100:103]\n"
    "ds_read_b128 a[44:47], v48 offset:2048\n"
    "v_mfma_f32_16x16x32_bf16 v[88:91], a[32:35], a[48:51], v[88:91]\n"
    "ds_read_b128 a[12:15], v46 offset:2048\n"
    "v_mfma_f32_16x16x32_bf16 v[104:107], a[32:35], a[52:55], v[104:107]\n"
    "ds_read_b128 a[16:19], v46 offset:4096\n"
    "v_mfma_f32_16x16x32_bf16 v[92:95], a[36:39], a[48:51], v[92:95]\n"
    "buffer_load_dwordx4 v11, s[4:7], s76 offen offset:1024 lds\n"
    "v_mfma_f32_16x16x32_bf16 v[108:111], a[36:39], a[52:55], v[108:111]\n"
    "ds_read_b128 a[20:23], v46 offset:6144\n"
    "v_mfma_f32_16x16x32_bf16 v[112:115], a[72:75], a[24:27], v[112:115]\n"
    "ds_read_b64_tr_b16 a[56:57], v50 offset:0\n"
    "v_mfma_f32_16x16x32_bf16 v[128:131], a[76:79], a[24:27], v[128:131]\n"
    "ds_read_b64_tr_b16 a[58:59], v50 offset:1024\n"
    "v_mfma_f32_16x16x32_bf16 v[116:119], a[72:75], a[28:31], v[116:119]\n"
    "ds_read_b64_tr_b16 a[60:61], v51 offset:0\n"
    "v_mfma_f32_16x16x32_bf16 v[132:135], a[76:79], a[28:31], v[132:135]\n"
    "buffer_load_dwordx4 v12, s[4:7], s76 offen offset:2048 lds\n"
    "v_mfma_f32_16x16x32_bf16 v[120:123], a[72:75], a[32:35], v[120:123]\n"
    "ds_read_b64_tr_b16 a[62:63], v51 offset:1024\n"
    "v_mfma_f32_16x16x32_bf16 v[136:139], a[76:79], a[32:35], v[136:139]\n"
    "ds_read_b64_tr_b16 a[64:65], v52 offset:0\n"
    "v_mfma_f32_16x16x32_bf16 v[124:127], a[72:75], a[36:39], v[124:127]\n"
    "ds_read_b64_tr_b16 a[66:67], v52 offset:1024\n"
    "v_mfma_f32_16x16x32_bf16 v[140:143], a[76:79], a[36:39], v[140:143]\n"
    "ds_read_b64_tr_b16 a[68:69], v53 offset:0\n"
    "v_mfma_f32_16x16x32_bf16 v[144:147], a[80:83], a[24:27], v[144:147]\n"
    "buffer_load_dwordx4 v13, s[4:7], s76 offen offset:3072 lds\n"
    "v_mfma_f32_16x16x32_bf16 v[160:163], a[84:87], a[24:27], v[160:163]\n"
    "ds_read_b64_tr_b16 a[70:71], v53 offset:1024\n"
    "v_mfma_f32_16x16x32_bf16 v[148:151], a[80:83], a[28:31], v[148:151]\n"
    "v_rcp_f32 v80, v80\n"
    "v_mfma_f32_16x16x32_bf16 v[164:167], a[84:87], a[28:31], v[164:167]\n"
    "s_add_u32 m0, s74, 49152\n"
    "v_mul_f32 v86, v78, v86\n"
    "v_mul_f32 v87, v78, v87\n"
    "v_mfma_f32_16x16x32_bf16 v[152:155], a[80:83], a[32:35], v[152:155]\n"
    "v_add_f32 v81, -1.0, v86\n"
    "v_mul_f32 v184, v81, v42\n"
    "v_mfma_f32_16x16x32_bf16 v[168:171], a[84:87], a[32:35], v[168:171]\n"
    "buffer_load_dwordx4 v14, s[8:11], s76 offen offset:0 lds\n"
    "v_mfma_f32_16x16x32_bf16 v[156:159], a[80:83], a[36:39], v[156:159]\n"
    "v_mul_f32 v185, v87, v42\n"
    "v_fmac_f32 v184, v87, v43\n"
    "v_mfma_f32_16x16x32_bf16 v[172:175], a[84:87], a[36:39], v[172:175]\n"
    "v_fma_f32 v185, -v81, v43, v185\n"
    "v_add_f32 v80, v80, v80\n"
    "s_waitcnt lgkmcnt(8)\n"
    "v_mfma_f32_16x16x32_bf16 v[28:31], a[8:11], a[40:43], v[28:31]\n"
    "ds_read_b128 a[24:27], v47 offset:0\n"
    "v_mul_f32 v184, v80, v184\n"
    "v_mfma_f32_16x16x32_bf16 v[96:99], a[8:11], a[44:47], v[96:99]\n"
    "ds_read_b128 a[48:51], v49 offset:0\n"
    "v_mul_f32 v185, v80, v185\n"
    "v_mfma_f32_16x16x32_bf16 v[72:75], a[12:15], a[40:43], v[72:75]\n"
    "ds_read_b128 a[52:55], v49 offset:2048\n"
    "v_mul_f32 v186, -1.0, v185\n"
    "v_mfma_f32_16x16x32_bf16 v[100:103], a[12:15], a[44:47], v[100:103]\n"
    "buffer_load_dwordx4 v15, s[8:11], s76 offen offset:1024 lds\n"
    "v_mfma_f32_16x16x32_bf16 v[88:91], a[16:19], a[40:43], v[88:91]\n"
    "ds_read_b128 a[28:31], v47 offset:2048\n"
    "v_mov_b32 v187, v184\n"
    "v_mfma_f32_16x16x32_bf16 v[104:107], a[16:19], a[44:47], v[104:107]\n"
    "ds_read_b128 a[32:35], v47 offset:4096\n"
    "v_mul_f32 v78, v87, v87\n"
    "v_mfma_f32_16x16x32_bf16 v[92:95], a[20:23], a[40:43], v[92:95]\n"
    "s_mov_b32 s77, 1146880\n"
    "ds_read_b128 a[36:39], v47 offset:6144\n"
    "s_add_u32 m0, s75, 32768\n"
    "v_mfma_f32_16x16x32_bf16 v[108:111], a[20:23], a[44:47], v[108:111]\n"
    "ds_read_b64_tr_b16 a[72:73], v54 offset:0\n"
    "v_mul_f32 v79, v86, v87\n"
    "v_mfma_f32_16x16x32_bf16 a[104:107], a[4:7], a[40:43], a[104:107]\n"
    "buffer_load_dwordx4 v16, s[12:15], s77 offen offset:0 lds\n"
    "v_mfma_f32_16x16x32_bf16 a[108:111], a[4:7], a[44:47], a[108:111]\n"
    "s_waitcnt lgkmcnt(11)\n"
    "ds_read_b64_tr_b16 a[74:75], v54 offset:1024\n"
    "v_fma_f32 v78, v86, v86, -v78\n"
    "v_mfma_f32_16x16x32_bf16 a[88:91], a[4:7], a[56:59], a[88:91]\n"
    "ds_read_b64_tr_b16 a[76:77], v55 offset:0\n"
    "v_add_f32 v79, v79, v79\n"
    "v_mfma_f32_16x16x32_bf16 a[92:95], a[4:7], a[60:63], a[92:95]\n"
    "ds_read_b64_tr_b16 a[78:79], v55 offset:1024\n"
    "v_mul_f32 v80, v79, v79\n"
    "v_mfma_f32_16x16x32_bf16 v[112:115], a[56:59], a[8:11], v[112:115]\n"
    "ds_read_b64_tr_b16 a[80:81], v56 offset:0\n"
    "v_mul_f32 v81, v78, v79\n"
    "v_mfma_f32_16x16x32_bf16 v[128:131], a[60:63], a[8:11], v[128:131]\n"
    "buffer_load_dwordx4 v17, s[12:15], s77 offen offset:1024 lds\n"
    "v_mfma_f32_16x16x32_bf16 v[116:119], a[56:59], a[12:15], v[116:119]\n"
    "ds_read_b64_tr_b16 a[82:83], v56 offset:1024\n"
    "v_fma_f32 v80, v78, v78, -v80\n"
    "v_mfma_f32_16x16x32_bf16 v[132:135], a[60:63], a[12:15], v[132:135]\n"
    "ds_read_b64_tr_b16 a[84:85], v57 offset:0\n"
    "v_add_f32 v81, v81, v81\n"
    "v_mfma_f32_16x16x32_bf16 v[120:123], a[56:59], a[16:19], v[120:123]\n"
    "ds_read_b64_tr_b16 a[86:87], v57 offset:1024\n"
    "v_mul_f32 v78, v81, v81\n"
    "v_mfma_f32_16x16x32_bf16 v[136:139], a[60:63], a[16:19], v[136:139]\n"
    "v_mul_f32 v79, v80, v81\n"
    "v_fma_f32 v78, v80, v80, -v78\n"
    "v_mfma_f32_16x16x32_bf16 v[124:127], a[56:59], a[20:23], v[124:127]\n"
    "buffer_load_dwordx4 v18, s[12:15], s77 offen offset:2048 lds\n"
    "v_mfma_f32_16x16x32_bf16 v[140:143], a[60:63], a[20:23], v[140:143]\n"
    "s_waitcnt lgkmcnt(14)\n"
    "v_add_f32 v79, v79, v79\n"
    "v_mul_f32 v176, v79, v79\n"
    "v_mfma_f32_16x16x32_bf16 a[96:99], a[4:7], a[64:67], a[96:99]\n"
    "v_mul_f32 v177, v78, v79\n"
    "v_fma_f32 v176, v78, v78, -v176\n"
    "v_mfma_f32_16x16x32_bf16 a[100:103], a[4:7], a[68:71], a[100:103]\n"
    "v_add_f32 v177, v177, v177\n"
    "v_mul_f32 v78, v177, v177\n"
    "v_mfma_f32_16x16x32_bf16 v[144:147], a[64:67], a[8:11], v[144:147]\n"
    "v_mul_f32 v79, v176, v177\n"
    "v_fma_f32 v78, v176, v176, -v78\n"
    "v_mfma_f32_16x16x32_bf16 v[160:163], a[68:71], a[8:11], v[160:163]\n"
    "buffer_load_dwordx4 v19, s[12:15], s77 offen offset:3072 lds\n"
    "v_mfma_f32_16x16x32_bf16 v[148:151], a[64:67], a[12:15], v[148:151]\n"
    "v_add_f32 v79, v79, v79\n"
    "v_mul_f32 v178, v79, v79\n"
    "v_mfma_f32_16x16x32_bf16 v[164:167], a[68:71], a[12:15], v[164:167]\n"
    "v_mul_f32 v179, v78, v79\n"
    "v_fma_f32 v178, v78, v78, -v178\n"
    "v_mfma_f32_16x16x32_bf16 v[152:155], a[64:67], a[16:19], v[152:155]\n"
    "v_add_f32 v179, v179, v179\n"
    "v_cndmask_b32 v82, 1.0, v176, s[58:59]\n"
    "v_mfma_f32_16x16x32_bf16 v[168:171], a[68:71], a[16:19], v[168:171]\n"
    "v_cndmask_b32 v84, 1.0, v78, s[60:61]\n"
    "v_cndmask_b32 v83, 0, v177, s[58:59]\n"
    "v_mfma_f32_16x16x32_bf16 v[156:159], a[64:67], a[20:23], v[156:159]\n"
    "v_cndmask_b32 v85, 0, v79, s[60:61]\n"
    "v_mul_f32 v182, v83, v85\n"
    "v_mfma_f32_16x16x32_bf16 v[172:175], a[68:71], a[20:23], v[172:175]\n"
    "v_mul_f32 v183, v82, v85\n"
    "v_fma_f32 v182, v82, v84, -v182\n"
    "s_waitcnt vmcnt(11) lgkmcnt(0)\n"
    "s_barrier\n"
    "v_mfma_f32_16x16x32_bf16 v[28:31], a[24:27], a[48:51], v[28:31]\n"
    "s_movk_i32 s76, 768\n"
    "ds_read_b128 a[8:11], v46 offset:24576\n"
    "s_add_u32 m0, s73, 0\n"
    "v_mfma_f32_16x16x32_bf16 v[96:99], a[24:27], a[52:55], v[96:99]\n"
    "buffer_load_dwordx4 v10, s[4:7], s76 offen offset:0 lds\n"
    "v_mfma_f32_16x16x32_bf16 v[72:75], a[28:31], a[48:51], v[72:75]\n"
    "ds_read_b128 a[40:43], v48 offset:24576\n"
    "v_fmac_f32 v183, v83, v84\n"
    "v_mfma_f32_16x16x32_bf16 v[100:103], a[28:31], a[52:55], v[100:103]\n"
    "ds_read_b128 a[44:47], v48 offset:26624\n"
    "v_mul_f32 v78, v179, v179\n"
    "v_mfma_f32_16x16x32_bf16 v[88:91], a[32:35], a[48:51], v[88:91]\n"
    "ds_read_b128 a[12:15], v46 offset:26624\n"
    "v_mul_f32 v79, v178, v179\n"
    "v_mfma_f32_16x16x32_bf16 v[104:107], a[32:35], a[52:55], v[104:107]\n"
    "buffer_load_dwordx4 v11, s[4:7], s76 offen offset:1024 lds\n"
    "v_mfma_f32_16x16x32_bf16 v[92:95], a[36:39], a[48:51], v[92:95]\n"
    "ds_read_b128 a[16:19], v46 offset:28672\n"
    "v_fma_f32 v78, v178, v178, -v78\n"
    "v_mfma_f32_16x16x32_bf16 v[108:111], a[36:39], a[52:55], v[108:111]\n"
    "ds_read_b128 a[20:23], v46 offset:30720\n"
    "v_add_f32 v79, v79, v79\n"
    "v_mfma_f32_16x16x32_bf16 v[112:115], a[72:75], a[24:27], v[112:115]\n"
    "ds_read_b64_tr_b16 a[56:57], v50 offset:16384\n"
    "v_mul_f32 v80, v79, v79\n"
    "v_mfma_f32_16x16x32_bf16 v[128:131], a[76:79], a[24:27], v[128:131]\n"
    "ds_read_b64_tr_b16 a[58:59], v50 offset:17408\n"
    "v_mul_f32 v81, v78, v79\n"
    "v_mfma_f32_16x16x32_bf16 v[116:119], a[72:75], a[28:31], v[116:119]\n"
    "buffer_load_dwordx4 v12, s[4:7], s76 offen offset:2048 lds\n"
    "v_mfma_f32_16x16x32_bf16 v[132:135], a[76:79], a[28:31], v[132:135]\n"
    "ds_read_b64_tr_b16 a[60:61], v51 offset:16384\n"
    "v_fma_f32 v80, v78, v78, -v80\n"
    "v_mfma_f32_16x16x32_bf16 v[120:123], a[72:75], a[32:35], v[120:123]\n"
    "ds_read_b64_tr_b16 a[62:63], v51 offset:17408\n"
    "v_add_f32 v81, v81, v81\n"
    "v_mfma_f32_16x16x32_bf16 v[136:139], a[76:79], a[32:35], v[136:139]\n"
    "ds_read_b64_tr_b16 a[64:65], v52 offset:16384\n"
    "v_mul_f32 v78, v81, v81\n"
    "v_mfma_f32_16x16x32_bf16 v[124:127], a[72:75], a[36:39], v[124:127]\n"
    "ds_read_b64_tr_b16 a[66:67], v52 offset:17408\n"
    "v_mul_f32 v79, v80, v81\n"
    "v_mfma_f32_16x16x32_bf16 v[140:143], a[76:79], a[36:39], v[140:143]\n"
    "buffer_load_dwordx4 v13, s[4:7], s76 offen offset:3072 lds\n"
    "v_mfma_f32_16x16x32_bf16 v[144:147], a[80:83], a[24:27], v[144:147]\n"
    "ds_read_b64_tr_b16 a[68:69], v53 offset:16384\n"
    "v_fma_f32 v78, v80, v80, -v78\n"
    "v_mfma_f32_16x16x32_bf16 v[160:163], a[84:87], a[24:27], v[160:163]\n"
    "s_add_u32 m0, s74, 0\n"
    "ds_read_b64_tr_b16 a[70:71], v53 offset:17408\n"
    "v_add_f32 v79, v79, v79\n"
    "v_mfma_f32_16x16x32_bf16 v[148:151], a[80:83], a[28:31], v[148:151]\n"
    "v_mul_f32 v80, v79, v79\n"
    "v_mul_f32 v81, v78, v79\n"
    "v_mfma_f32_16x16x32_bf16 v[164:167], a[84:87], a[28:31], v[164:167]\n"
    "buffer_load_dwordx4 v14, s[8:11], s76 offen offset:0 lds\n"
    "v_mfma_f32_16x16x32_bf16 v[152:155], a[80:83], a[32:35], v[152:155]\n"
    "v_fma_f32 v80, v78, v78, -v80\n"
    "v_add_f32 v81, v81, v81\n"
    "v_mfma_f32_16x16x32_bf16 v[168:171], a[84:87], a[32:35], v[168:171]\n"
    "v_cndmask_b32 v82, 1.0, v78, s[58:59]\n"
    "v_cndmask_b32 v84, 1.0, v80, s[60:61]\n"
    "v_mfma_f32_16x16x32_bf16 v[156:159], a[80:83], a[36:39], v[156:159]\n"
    "v_cndmask_b32 v83, 0, v79, s[58:59]\n"
    "v_cndmask_b32 v85, 0, v81, s[60:61]\n"
    "v_mfma_f32_16x16x32_bf16 v[172:175], a[84:87], a[36:39], v[172:175]\n"
    "v_mul_f32 v180, v83, v85\n"
    "v_mul_f32 v181, v82, v85\n"
    "s_waitcnt lgkmcnt(8)\n"
    "v_mfma_f32_16x16x32_bf16 v[28:31], a[8:11], a[40:43], v[28:31]\n"
    "buffer_load_dwordx4 v15, s[8:11], s76 offen offset:1024 lds\n"
    "v_mfma_f32_16x16x32_bf16 v[96:99], a[8:11], a[44:47], v[96:99]\n"
    "ds_read_b128 a[24:27], v47 offset:24576\n"
    "v_fma_f32 v180, v82, v84, -v180\n"
    "v_mfma_f32_16x16x32_bf16 v[72:75], a[12:15], a[40:43], v[72:75]\n"
    "ds_read_b128 a[48:51], v49 offset:24576\n"
    "v_fmac_f32 v181, v83, v84\n"
    "v_mfma_f32_16x16x32_bf16 v[100:103], a[12:15], a[44:47], v[100:103]\n"
    "s_mov_b32 s77, 1376256\n"
    "ds_read_b128 a[52:55], v49 offset:26624\n"
    "s_add_u32 m0, s75, 0\n"
    "v_mfma_f32_16x16x32_bf16 v[88:91], a[16:19], a[40:43], v[88:91]\n"
    "ds_read_b128 a[28:31], v47 offset:26624\n"
    "v_lshlrev_b32 v188, 16, v32\n"
    "v_mfma_f32_16x16x32_bf16 v[104:107], a[16:19], a[44:47], v[104:107]\n"
    "buffer_load_dwordx4 v16, s[12:15], s77 offen offset:0 lds\n"
    "v_mfma_f32_16x16x32_bf16 v[92:95], a[20:23], a[40:43], v[92:95]\n"
    "ds_read_b128 a[32:35], v47 offset:28672\n"
    "v_and_b32 v189, 0xffff0000, v32\n"
    "v_mfma_f32_16x16x32_bf16 v[108:111], a[20:23], a[44:47], v[108:111]\n"
    "ds_read_b128 a[36:39], v47 offset:30720\n"
    "v_lshlrev_b32 v190, 16, v33\n"
    "v_mfma_f32_16x16x32_bf16 a[104:107], a[4:7], a[40:43], a[104:107]\n"
    "ds_read_b64_tr_b16 a[72:73], v54 offset:16384\n"
    "v_and_b32 v191, 0xffff0000, v33\n"
    "v_mfma_f32_16x16x32_bf16 a[108:111], a[4:7], a[44:47], a[108:111]\n"
    "s_waitcnt lgkmcnt(11)\n"
    "ds_read_b64_tr_b16 a[74:75], v54 offset:17408\n"
    "v_lshlrev_b32 v192, 16, v34\n"
    "v_mfma_f32_16x16x32_bf16 a[88:91], a[4:7], a[56:59], a[88:91]\n"
    "buffer_load_dwordx4 v17, s[12:15], s77 offen offset:1024 lds\n"
    "v_mfma_f32_16x16x32_bf16 a[92:95], a[4:7], a[60:63], a[92:95]\n"
    "ds_read_b64_tr_b16 a[76:77], v55 offset:16384\n"
    "v_and_b32 v193, 0xffff0000, v34\n"
    "v_mfma_f32_16x16x32_bf16 v[112:115], a[56:59], a[8:11], v[112:115]\n"
    "ds_read_b64_tr_b16 a[78:79], v55 offset:17408\n"
    "v_lshlrev_b32 v194, 16, v35\n"
    "v_mfma_f32_16x16x32_bf16 v[128:131], a[60:63], a[8:11], v[128:131]\n"
    "ds_read_b64_tr_b16 a[80:81], v56 offset:16384\n"
    "v_and_b32 v195, 0xffff0000, v35\n"
    "v_mfma_f32_16x16x32_bf16 v[116:119], a[56:59], a[12:15], v[116:119]\n"
    "buffer_load_dwordx4 v18, s[12:15], s77 offen offset:2048 lds\n"
    "v_mfma_f32_16x16x32_bf16 v[132:135], a[60:63], a[12:15], v[132:135]\n"
    "ds_read_b64_tr_b16 a[82:83], v56 offset:17408\n"
    "v_lshlrev_b32 v196, 16, v36\n"
    "v_mfma_f32_16x16x32_bf16 v[120:123], a[56:59], a[16:19], v[120:123]\n"
    "ds_read_b64_tr_b16 a[84:85], v57 offset:16384\n"
    "v_and_b32 v197, 0xffff0000, v36\n"
    "v_mfma_f32_16x16x32_bf16 v[136:139], a[60:63], a[16:19], v[136:139]\n"
    "ds_read_b64_tr_b16 a[86:87], v57 offset:17408\n"
    "v_lshlrev_b32 v198, 16, v37\n"
    "v_mfma_f32_16x16x32_bf16 v[124:127], a[56:59], a[20:23], v[124:127]\n"
    "v_and_b32 v199, 0xffff0000, v37\n"
    "v_lshlrev_b32 v200, 16, v38\n"
    "v_mfma_f32_16x16x32_bf16 v[140:143], a[60:63], a[20:23], v[140:143]\n"
    "s_waitcnt lgkmcnt(14)\n"
    "buffer_load_dwordx4 v19, s[12:15], s77 offen offset:3072 lds\n"
    "v_mfma_f32_16x16x32_bf16 a[96:99], a[4:7], a[64:67], a[96:99]\n"
    "v_and_b32 v201, 0xffff0000, v38\n"
    "v_lshlrev_b32 v202, 16, v39\n"
    "v_mfma_f32_16x16x32_bf16 a[100:103], a[4:7], a[68:71], a[100:103]\n"
    "v_and_b32 v203, 0xffff0000, v39\n"
    "v_mfma_f32_16x16x32_bf16 v[144:147], a[64:67], a[8:11], v[144:147]\n"
    "s_lshl_b32 s54, s66, 10\n"
    "s_add_u32 s54, s54, 4096\n"
    "v_mfma_f32_16x16x32_bf16 v[160:163], a[68:71], a[8:11], v[160:163]\n"
    "v_mfma_f32_16x16x32_bf16 v[148:151], a[64:67], a[12:15], v[148:151]\n"
    "buffer_load_dwordx4 v[24:27], v3, s[16:19], s54 offen sc1\n"
    "v_mfma_f32_16x16x32_bf16 v[164:167], a[68:71], a[12:15], v[164:167]\n"
    "v_mfma_f32_16x16x32_bf16 v[152:155], a[64:67], a[16:19], v[152:155]\n"
    "v_mfma_f32_16x16x32_bf16 v[168:171], a[68:71], a[16:19], v[168:171]\n"
    "v_mfma_f32_16x16x32_bf16 v[156:159], a[64:67], a[20:23], v[156:159]\n"
    "v_mfma_f32_16x16x32_bf16 v[172:175], a[68:71], a[20:23], v[172:175]\n"
    "s_waitcnt vmcnt(11) lgkmcnt(0)\n"
    "s_barrier\n"
    "v_mfma_f32_16x16x32_bf16 v[28:31], a[24:27], a[48:51], v[28:31]\n"
    "s_movk_i32 s76, 896\n"
    "ds_read_b128 a[8:11], v46 offset:49152\n"
    "s_add_u32 m0, s73, 24576\n"
    "v_mfma_f32_16x16x32_bf16 v[96:99], a[24:27], a[52:55], v[96:99]\n"
    "buffer_load_dwordx4 v10, s[4:7], s76 offen offset:0 lds\n"
    "v_mfma_f32_16x16x32_bf16 v[72:75], a[28:31], a[48:51], v[72:75]\n"
    "ds_read_b128 a[40:43], v48 offset:49152\n"
    "v_mfma_f32_16x16x32_bf16 v[100:103], a[28:31], a[52:55], v[100:103]\n"
    "ds_read_b128 a[44:47], v48 offset:51200\n"
    "v_mfma_f32_16x16x32_bf16 v[88:91], a[32:35], a[48:51], v[88:91]\n"
    "ds_read_b128 a[12:15], v46 offset:51200\n"
    "v_mfma_f32_16x16x32_bf16 v[104:107], a[32:35], a[52:55], v[104:107]\n"
    "buffer_load_dwordx4 v11, s[4:7], s76 offen offset:1024 lds\n"
    "v_mfma_f32_16x16x32_bf16 v[92:95], a[36:39], a[48:51], v[92:95]\n"
    "ds_read_b128 a[16:19], v46 offset:53248\n"
    "v_mfma_f32_16x16x32_bf16 v[108:111], a[36:39], a[52:55], v[108:111]\n"
    "ds_read_b128 a[20:23], v46 offset:55296\n"
    "v_mfma_f32_16x16x32_bf16 v[112:115], a[72:75], a[24:27], v[112:115]\n"
    "ds_read_b64_tr_b16 a[56:57], v50 offset:32768\n"
    "v_mfma_f32_16x16x32_bf16 v[128:131], a[76:79], a[24:27], v[128:131]\n"
    "buffer_load_dwordx4 v12, s[4:7], s76 offen offset:2048 lds\n"
    "v_mfma_f32_16x16x32_bf16 v[116:119], a[72:75], a[28:31], v[116:119]\n"
    "ds_read_b64_tr_b16 a[58:59], v50 offset:33792\n"
    "v_mfma_f32_16x16x32_bf16 v[132:135], a[76:79], a[28:31], v[132:135]\n"
    "ds_read_b64_tr_b16 a[60:61], v51 offset:32768\n"
    "v_mfma_f32_16x16x32_bf16 v[120:123], a[72:75], a[32:35], v[120:123]\n"
    "ds_read_b64_tr_b16 a[62:63], v51 offset:33792\n"
    "v_mfma_f32_16x16x32_bf16 v[136:139], a[76:79], a[32:35], v[136:139]\n"
    "ds_read_b64_tr_b16 a[64:65], v52 offset:32768\n"
    "v_mfma_f32_16x16x32_bf16 v[124:127], a[72:75], a[36:39], v[124:127]\n"
    "buffer_load_dwordx4 v13, s[4:7], s76 offen offset:3072 lds\n"
    "v_mfma_f32_16x16x32_bf16 v[140:143], a[76:79], a[36:39], v[140:143]\n"
    "ds_read_b64_tr_b16 a[66:67], v52 offset:33792\n"
    "v_mfma_f32_16x16x32_bf16 v[144:147], a[80:83], a[24:27], v[144:147]\n"
    "s_add_u32 m0, s74, 24576\n"
    "ds_read_b64_tr_b16 a[68:69], v53 offset:32768\n"
    "v_mfma_f32_16x16x32_bf16 v[160:163], a[84:87], a[24:27], v[160:163]\n"
    "ds_read_b64_tr_b16 a[70:71], v53 offset:33792\n"
    "v_mfma_f32_16x16x32_bf16 v[148:151], a[80:83], a[28:31], v[148:151]\n"
    "buffer_load_dwordx4 v14, s[8:11], s76 offen offset:0 lds\n"
    "v_mfma_f32_16x16x32_bf16 v[164:167], a[84:87], a[28:31], v[164:167]\n"
    "v_mfma_f32_16x16x32_bf16 v[152:155], a[80:83], a[32:35], v[152:155]\n"
    "v_mfma_f32_16x16x32_bf16 v[168:171], a[84:87], a[32:35], v[168:171]\n"
    "v_mfma_f32_16x16x32_bf16 v[156:159], a[80:83], a[36:39], v[156:159]\n"
    "buffer_load_dwordx4 v15, s[8:11], s76 offen offset:1024 lds\n"
    "v_mfma_f32_16x16x32_bf16 v[172:175], a[84:87], a[36:39], v[172:175]\n"
    "s_waitcnt lgkmcnt(8)\n"
    "v_mfma_f32_16x16x32_bf16 v[28:31], a[8:11], a[40:43], v[28:31]\n"
    "ds_read_b128 a[24:27], v47 offset:49152\n"
    "v_mfma_f32_16x16x32_bf16 v[96:99], a[8:11], a[44:47], v[96:99]\n"
    "s_mov_b32 s77, 1605632\n"
    "ds_read_b128 a[48:51], v49 offset:49152\n"
    "s_add_u32 m0, s75, 16384\n"
    "v_mfma_f32_16x16x32_bf16 v[72:75], a[12:15], a[40:43], v[72:75]\n"
    "ds_read_b128 a[52:55], v49 offset:51200\n"
    "v_mfma_f32_16x16x32_bf16 v[100:103], a[12:15], a[44:47], v[100:103]\n"
    "buffer_load_dwordx4 v16, s[12:15], s77 offen offset:0 lds\n"
    "v_mfma_f32_16x16x32_bf16 v[88:91], a[16:19], a[40:43], v[88:91]\n"
    "ds_read_b128 a[28:31], v47 offset:51200\n"
    "v_mfma_f32_16x16x32_bf16 v[104:107], a[16:19], a[44:47], v[104:107]\n"
    "ds_read_b128 a[32:35], v47 offset:53248\n"
    "v_mfma_f32_16x16x32_bf16 v[92:95], a[20:23], a[40:43], v[92:95]\n"
    "ds_read_b128 a[36:39], v47 offset:55296\n"
    "v_mfma_f32_16x16x32_bf16 v[108:111], a[20:23], a[44:47], v[108:111]\n"
    "buffer_load_dwordx4 v17, s[12:15], s77 offen offset:1024 lds\n"
    "v_mfma_f32_16x16x32_bf16 a[104:107], a[4:7], a[40:43], a[104:107]\n"
    "ds_read_b64_tr_b16 a[72:73], v54 offset:32768\n"
    "v_mfma_f32_16x16x32_bf16 a[108:111], a[4:7], a[44:47], a[108:111]\n"
    "s_waitcnt lgkmcnt(11)\n"
    "ds_read_b64_tr_b16 a[74:75], v54 offset:33792\n"
    "v_mfma_f32_16x16x32_bf16 a[88:91], a[4:7], a[56:59], a[88:91]\n"
    "ds_read_b64_tr_b16 a[76:77], v55 offset:32768\n"
    "v_mfma_f32_16x16x32_bf16 a[92:95], a[4:7], a[60:63], a[92:95]\n"
    "buffer_load_dwordx4 v18, s[12:15], s77 offen offset:2048 lds\n"
    "v_mfma_f32_16x16x32_bf16 v[112:115], a[56:59], a[8:11], v[112:115]\n"
    "ds_read_b64_tr_b16 a[78:79], v55 offset:33792\n"
    "v_mfma_f32_16x16x32_bf16 v[128:131], a[60:63], a[8:11], v[128:131]\n"
    "ds_read_b64_tr_b16 a[80:81], v56 offset:32768\n"
    "v_mfma_f32_16x16x32_bf16 v[116:119], a[56:59], a[12:15], v[116:119]\n"
    "ds_read_b64_tr_b16 a[82:83], v56 offset:33792\n"
    "v_mfma_f32_16x16x32_bf16 v[132:135], a[60:63], a[12:15], v[132:135]\n"
    "ds_read_b64_tr_b16 a[84:85], v57 offset:32768\n"
    "v_mfma_f32_16x16x32_bf16 v[120:123], a[56:59], a[16:19], v[120:123]\n"
    "buffer_load_dwordx4 v19, s[12:15], s77 offen offset:3072 lds\n"
    "v_mfma_f32_16x16x32_bf16 v[136:139], a[60:63], a[16:19], v[136:139]\n"
    "ds_read_b64_tr_b16 a[86:87], v57 offset:33792\n"
    "v_mfma_f32_16x16x32_bf16 v[124:127], a[56:59], a[20:23], v[124:127]\n"
    "s_add_u32 s82, s83, 0\n"
    "s_add_u32 m0, s73, 122880\n"
    "v_mfma_f32_16x16x32_bf16 v[140:143], a[60:63], a[20:23], v[140:143]\n"
    "s_waitcnt lgkmcnt(14)\n"
    "v_mfma_f32_16x16x32_bf16 a[96:99], a[4:7], a[64:67], a[96:99]\n"
    "buffer_load_dwordx4 v61, s[20:23], s82 offen offset:0 lds\n"
    "v_mfma_f32_16x16x32_bf16 a[100:103], a[4:7], a[68:71], a[100:103]\n"
    "v_mfma_f32_16x16x32_bf16 v[144:147], a[64:67], a[8:11], v[144:147]\n"
    "v_mfma_f32_16x16x32_bf16 v[160:163], a[68:71], a[8:11], v[160:163]\n"
    "v_mfma_f32_16x16x32_bf16 v[148:151], a[64:67], a[12:15], v[148:151]\n"
    "buffer_load_dwordx4 v62, s[20:23], s82 offen offset:1024 lds\n"
    "v_mfma_f32_16x16x32_bf16 v[164:167], a[68:71], a[12:15], v[164:167]\n"
    "v_mfma_f32_16x16x32_bf16 v[152:155], a[64:67], a[16:19], v[152:155]\n"
    "v_mfma_f32_16x16x32_bf16 v[168:171], a[68:71], a[16:19], v[168:171]\n"
    "v_mfma_f32_16x16x32_bf16 v[156:159], a[64:67], a[20:23], v[156:159]\n"
    "v_mfma_f32_16x16x32_bf16 v[172:175], a[68:71], a[20:23], v[172:175]\n"
    "s_waitcnt vmcnt(13) lgkmcnt(0)\n"
    "s_barrier\n"
    "v_mfma_f32_16x16x32_bf16 v[28:31], a[24:27], a[48:51], v[28:31]\n"
    "ds_read_b128 a[8:11], v46 offset:0\n"
    "v_mfma_f32_16x16x32_bf16 v[96:99], a[24:27], a[52:55], v[96:99]\n"
    "buffer_load_dwordx4 v63, s[20:23], s82 offen offset:2048 lds\n"
    "v_mfma_f32_16x16x32_bf16 v[72:75], a[28:31], a[48:51], v[72:75]\n"
    "ds_read_b128 a[40:43], v48 offset:0\n"
    "v_mfma_f32_16x16x32_bf16 v[100:103], a[28:31], a[52:55], v[100:103]\n"
    "ds_read_b128 a[44:47], v48 offset:2048\n"
    "v_mfma_f32_16x16x32_bf16 v[88:91], a[32:35], a[48:51], v[88:91]\n"
    "ds_read_b128 a[12:15], v46 offset:2048\n"
    "v_mfma_f32_16x16x32_bf16 v[104:107], a[32:35], a[52:55], v[104:107]\n"
    "buffer_load_dwordx4 v64, s[20:23], s82 offen offset:3072 lds\n"
    "v_mfma_f32_16x16x32_bf16 v[92:95], a[36:39], a[48:51], v[92:95]\n"
    "ds_read_b128 a[16:19], v46 offset:4096\n"
    "v_mfma_f32_16x16x32_bf16 v[108:111], a[36:39], a[52:55], v[108:111]\n"
    "s_mov_b32 s82, 0\n"
    "ds_read_b128 a[20:23], v46 offset:6144\n"
    "s_add_u32 m0, s73, 139264\n"
    "v_mfma_f32_16x16x32_bf16 v[112:115], a[72:75], a[24:27], v[112:115]\n"
    "ds_read_b64_tr_b16 a[56:57], v50 offset:0\n"
    "v_mfma_f32_16x16x32_bf16 v[128:131], a[76:79], a[24:27], v[128:131]\n"
    "buffer_load_dwordx4 v65, s[24:27], s82 offen offset:0 lds\n"
    "v_mfma_f32_16x16x32_bf16 v[116:119], a[72:75], a[28:31], v[116:119]\n"
    "ds_read_b64_tr_b16 a[58:59], v50 offset:1024\n"
    "v_mfma_f32_16x16x32_bf16 v[132:135], a[76:79], a[28:31], v[132:135]\n"
    "ds_read_b64_tr_b16 a[60:61], v51 offset:0\n"
    "v_mfma_f32_16x16x32_bf16 v[120:123], a[72:75], a[32:35], v[120:123]\n"
    "ds_read_b64_tr_b16 a[62:63], v51 offset:1024\n"
    "v_mfma_f32_16x16x32_bf16 v[136:139], a[76:79], a[32:35], v[136:139]\n"
    "ds_read_b64_tr_b16 a[64:65], v52 offset:0\n"
    "v_mfma_f32_16x16x32_bf16 v[124:127], a[72:75], a[36:39], v[124:127]\n"
    "buffer_load_dwordx4 v66, s[24:27], s82 offen offset:1024 lds\n"
    "v_mfma_f32_16x16x32_bf16 v[140:143], a[76:79], a[36:39], v[140:143]\n"
    "ds_read_b64_tr_b16 a[66:67], v52 offset:1024\n"
    "v_mfma_f32_16x16x32_bf16 v[144:147], a[80:83], a[24:27], v[144:147]\n"
    "ds_read_b64_tr_b16 a[68:69], v53 offset:0\n"
    "v_mfma_f32_16x16x32_bf16 v[160:163], a[84:87], a[24:27], v[160:163]\n"
    "ds_read_b64_tr_b16 a[70:71], v53 offset:1024\n"
    "v_mfma_f32_16x16x32_bf16 v[148:151], a[80:83], a[28:31], v[148:151]\n"
    "buffer_load_dwordx4 v67, s[24:27], s82 offen offset:2048 lds\n"
    "v_mfma_f32_16x16x32_bf16 v[164:167], a[84:87], a[28:31], v[164:167]\n"
    "v_mfma_f32_16x16x32_bf16 v[152:155], a[80:83], a[32:35], v[152:155]\n"
    "v_mfma_f32_16x16x32_bf16 v[168:171], a[84:87], a[32:35], v[168:171]\n"
    "v_mfma_f32_16x16x32_bf16 v[156:159], a[80:83], a[36:39], v[156:159]\n"
    "buffer_load_dwordx4 v68, s[24:27], s82 offen offset:3072 lds\n"
    "v_mfma_f32_16x16x32_bf16 v[172:175], a[84:87], a[36:39], v[172:175]\n"
    "s_waitcnt lgkmcnt(8)\n"
    "v_mfma_f32_16x16x32_bf16 v[28:31], a[8:11], a[40:43], v[28:31]\n"
    "s_waitcnt vmcnt(18)\n"
    "ds_read_b128 a[24:27], v47 offset:0\n"
    "v_cmp_ne_u64 vcc, v[26:27], s[56:57]\n"
    "v_mfma_f32_16x16x32_bf16 v[96:99], a[8:11], a[44:47], v[96:99]\n"
    "s_add_u32 s82, s83, 32768\n"
    "ds_read_b128 a[48:51], v49 offset:0\n"
    "s_add_u32 m0, s73, 49152\n"
    "v_mfma_f32_16x16x32_bf16 v[72:75], a[12:15], a[40:43], v[72:75]\n"
    "s_cbranch_vccnz .Lstr1_%=\n"
    ".Lstb2_%=:\n"
    "ds_read_b128 a[52:55], v49 offset:2048\n"
    "v_mfma_f32_16x16x32_bf16 v[100:103], a[12:15], a[44:47], v[100:103]\n"
    "buffer_load_dwordx4 v61, s[20:23], s82 offen offset:0 lds\n"
    "v_mfma_f32_16x16x32_bf16 v[88:91], a[16:19], a[40:43], v[88:91]\n"
    "ds_read_b128 a[28:31], v47 offset:2048\n"
    "v_mfma_f32_16x16x32_bf16 v[104:107], a[16:19], a[44:47], v[104:107]\n"
    "ds_read_b128 a[32:35], v47 offset:4096\n"
    "v_mfma_f32_16x16x32_bf16 v[92:95], a[20:23], a[40:43], v[92:95]\n"
    "ds_read_b128 a[36:39], v47 offset:6144\n"
    "v_mfma_f32_16x16x32_bf16 v[108:111], a[20:23], a[44:47], v[108:111]\n"
    "buffer_load_dwordx4 v62, s[20:23], s82 offen offset:1024 lds\n"
    "v_mfma_f32_16x16x32_bf16 a[104:107], a[4:7], a[40:43], a[104:107]\n"
    "ds_read_b64_tr_b16 a[72:73], v54 offset:0\n"
    "v_mfma_f32_16x16x32_bf16 a[108:111], a[4:7], a[44:47], a[108:111]\n"
    "s_waitcnt lgkmcnt(11)\n"
    "ds_read_b64_tr_b16 a[74:75], v54 offset:1024\n"
    "v_mfma_f32_16x16x32_bf16 a[88:91], a[4:7], a[56:59], a[88:91]\n"
    "ds_read_b64_tr_b16 a[76:77], v55 offset:0\n"
    "v_mfma_f32_16x16x32_bf16 a[92:95], a[4:7], a[60:63], a[92:95]\n"
    "buffer_load_dwordx4 v63, s[20:23], s82 offen offset:2048 lds\n"
    "v_mfma_f32_16x16x32_bf16 v[112:115], a[56:59], a[8:11], v[112:115]\n"
    "ds_read_b64_tr_b16 a[78:79], v55 offset:1024\n"
    "v_mfma_f32_16x16x32_bf16 v[128:131], a[60:63], a[8:11], v[128:131]\n"
    "ds_read_b64_tr_b16 a[80:81], v56 offset:0\n"
    "v_mfma_f32_16x16x32_bf16 v[116:119], a[56:59], a[12:15], v[116:119]\n"
    "ds_read_b64_tr_b16 a[82:83], v56 offset:1024\n"
    "v_mfma_f32_16x16x32_bf16 v[132:135], a[60:63], a[12:15], v[132:135]\n"
    "ds_read_b64_tr_b16 a[84:85], v57 offset:0\n"
    "v_mfma_f32_16x16x32_bf16 v[120:123], a[56:59], a[16:19], v[120:123]\n"
    "buffer_load_dwordx4 v64, s[20:23], s82 offen offset:3072 lds\n"
    "v_mfma_f32_16x16x32_bf16 v[136:139], a[60:63], a[16:19], v[136:139]\n"
    "ds_read_b64_tr_b16 a[86:87], v57 offset:1024\n"
    "v_mfma_f32_16x16x32_bf16 v[124:127], a[56:59], a[20:23], v[124:127]\n"
    "s_add_u32 s82, s83, 65536\n"
    "s_add_u32 m0, s73, 106496\n"
    "ds_write_b64 v22, v[24:25]\n"
    "v_mfma_f32_16x16x32_bf16 v[140:143], a[60:63], a[20:23], v[140:143]\n"
    "s_waitcnt lgkmcnt(15)\n"
    "v_mfma_f32_16x16x32_bf16 a[96:99], a[4:7], a[64:67], a[96:99]\n"
    "buffer_load_dwordx4 v61, s[20:23], s82 offen offset:0 lds\n"
    "v_mfma_f32_16x16x32_bf16 a[100:103], a[4:7], a[68:71], a[100:103]\n"
    "v_mfma_f32_16x16x32_bf16 v[144:147], a[64:67], a[8:11], v[144:147]\n"
    "v_mfma_f32_16x16x32_bf16 v[160:163], a[68:71], a[8:11], v[160:163]\n"
    "v_mfma_f32_16x16x32_bf16 v[148:151], a[64:67], a[12:15], v[148:151]\n"
    "buffer_load_dwordx4 v62, s[20:23], s82 offen offset:1024 lds\n"
    "v_mfma_f32_16x16x32_bf16 v[164:167], a[68:71], a[12:15], v[164:167]\n"
    "v_mfma_f32_16x16x32_bf16 v[152:155], a[64:67], a[16:19], v[152:155]\n"
    "v_mfma_f32_16x16x32_bf16 v[168:171], a[68:71], a[16:19], v[168:171]\n"
    "v_mfma_f32_16x16x32_bf16 v[156:159], a[64:67], a[20:23], v[156:159]\n"
    "v_mfma_f32_16x16x32_bf16 v[172:175], a[68:71], a[20:23], v[172:175]\n"
    "s_waitcnt vmcnt(14) lgkmcnt(0)\n"
    "s_barrier\n"
    "v_mfma_f32_16x16x32_bf16 v[28:31], a[24:27], a[48:51], v[28:31]\n"
    "ds_read_b128 a[8:11], v46 offset:24576\n"
    "v_mfma_f32_16x16x32_bf16 v[96:99], a[24:27], a[52:55], v[96:99]\n"
    "buffer_load_dwordx4 v63, s[20:23], s82 offen offset:2048 lds\n"
    "v_mfma_f32_16x16x32_bf16 v[72:75], a[28:31], a[48:51], v[72:75]\n"
    "ds_read_b128 a[40:43], v48 offset:24576\n"
    "v_mfma_f32_16x16x32_bf16 v[100:103], a[28:31], a[52:55], v[100:103]\n"
    "ds_read_b128 a[44:47], v48 offset:26624\n"
    "v_mfma_f32_16x16x32_bf16 v[88:91], a[32:35], a[48:51], v[88:91]\n"
    "ds_read_b128 a[12:15], v46 offset:26624\n"
    "v_mfma_f32_16x16x32_bf16 v[104:107], a[32:35], a[52:55], v[104:107]\n"
    "ds_read_b128 a[16:19], v46 offset:28672\n"
    "v_mfma_f32_16x16x32_bf16 v[92:95], a[36:39], a[48:51], v[92:95]\n"
    "buffer_load_dwordx4 v64, s[20:23], s82 offen offset:3072 lds\n"
    "v_mfma_f32_16x16x32_bf16 v[108:111], a[36:39], a[52:55], v[108:111]\n"
    "ds_read_b128 a[20:23], v46 offset:30720\n"
    "v_mfma_f32_16x16x32_bf16 v[112:115], a[72:75], a[24:27], v[112:115]\n"
    "ds_read_b64_tr_b16 a[56:57], v50 offset:16384\n"
    "v_mfma_f32_16x16x32_bf16 v[128:131], a[76:79], a[24:27], v[128:131]\n"
    "s_add_u32 s82, s83, 98304\n"
    "ds_read_b64_tr_b16 a[58:59], v50 offset:17408\n"
    "s_add_u32 m0, s73, 0\n"
    "v_mfma_f32_16x16x32_bf16 v[116:119], a[72:75], a[28:31], v[116:119]\n"
    "ds_read_b64_tr_b16 a[60:61], v51 offset:16384\n"
    "v_mfma_f32_16x16x32_bf16 v[132:135], a[76:79], a[28:31], v[132:135]\n"
    "buffer_load_dwordx4 v61, s[20:23], s82 offen offset:0 lds\n"
    "v_mfma_f32_16x16x32_bf16 v[120:123], a[72:75], a[32:35], v[120:123]\n"
    "ds_read_b64_tr_b16 a[62:63], v51 offset:17408\n"
    "v_mfma_f32_16x16x32_bf16 v[136:139], a[76:79], a[32:35], v[136:139]\n"
    "ds_read_b64_tr_b16 a[64:65], v52 offset:16384\n"
    "v_mfma_f32_16x16x32_bf16 v[124:127], a[72:75], a[36:39], v[124:127]\n"
    "ds_read_b64_tr_b16 a[66:67], v52 offset:17408\n"
    "v_mfma_f32_16x16x32_bf16 v[140:143], a[76:79], a[36:39], v[140:143]\n"
    "ds_read_b64_tr_b16 a[68:69], v53 offset:16384\n"
    "v_mfma_f32_16x16x32_bf16 v[144:147], a[80:83], a[24:27], v[144:147]\n"
    "buffer_load_dwordx4 v62, s[20:23], s82 offen offset:1024 lds\n"
    "v_mfma_f32_16x16x32_bf16 v[160:163], a[84:87], a[24:27], v[160:163]\n"
    "ds_read_b64_tr_b16 a[70:71], v53 offset:17408\n"
    "v_mfma_f32_16x16x32_bf16 v[148:151], a[80:83], a[28:31], v[148:151]\n"
    "v_mfma_f32_16x16x32_bf16 v[164:167], a[84:87], a[28:31], v[164:167]\n"
    "v_mfma_f32_16x16x32_bf16 v[152:155], a[80:83], a[32:35], v[152:155]\n"
    "v_mfma_f32_16x16x32_bf16 v[168:171], a[84:87], a[32:35], v[168:171]\n"
    "buffer_load_dwordx4 v63, s[20:23], s82 offen offset:2048 lds\n"
    "v_mfma_f32_16x16x32_bf16 v[156:159], a[80:83], a[36:39], v[156:159]\n"
    "v_mfma_f32_16x16x32_bf16 v[172:175], a[84:87], a[36:39], v[172:175]\n"
    "s_waitcnt lgkmcnt(8)\n"
    "v_mfma_f32_16x16x32_bf16 v[28:31], a[8:11], a[40:43], v[28:31]\n"
    "ds_read_b128 a[24:27], v47 offset:24576\n"
    "v_mfma_f32_16x16x32_bf16 v[96:99], a[8:11], a[44:47], v[96:99]\n"
    "ds_read_b128 a[48:51], v49 offset:24576\n"
    "v_mfma_f32_16x16x32_bf16 v[72:75], a[12:15], a[40:43], v[72:75]\n"
    "ds_read_b128 a[52:55], v49 offset:26624\n"
    "v_mfma_f32_16x16x32_bf16 v[100:103], a[12:15], a[44:47], v[100:103]\n"
    "buffer_load_dwordx4 v64, s[20:23], s82 offen offset:3072 lds\n"
    "v_mfma_f32_16x16x32_bf16 v[88:91], a[16:19], a[40:43], v[88:91]\n"
    "ds_read_b128 a[28:31], v47 offset:26624\n"
    "v_mfma_f32_16x16x32_bf16 v[104:107], a[16:19], a[44:47], v[104:107]\n"
    "ds_read_b128 a[32:35], v47 offset:28672\n"
    "v_mfma_f32_16x16x32_bf16 v[92:95], a[20:23], a[40:43], v[92:95]\n"
    "s_add_u32 s82, s83, 131072\n"
    "ds_read_b128 a[36:39], v47 offset:30720\n"
    "s_add_u32 m0, s73, 73728\n"
    "v_mfma_f32_16x16x32_bf16 v[108:111], a[20:23], a[44:47], v[108:111]\n"
    "ds_read_b64_tr_b16 a[72:73], v54 offset:16384\n"
    "v_mfma_f32_16x16x32_bf16 a[104:107], a[4:7], a[40:43], a[104:107]\n"
    "buffer_load_dwordx4 v61, s[20:23], s82 offen offset:0 lds\n"
    "v_mfma_f32_16x16x32_bf16 a[108:111], a[4:7], a[44:47], a[108:111]\n"
    "s_waitcnt lgkmcnt(11)\n"
    "ds_read_b64_tr_b16 a[74:75], v54 offset:17408\n"
    "v_mfma_f32_16x16x32_bf16 a[88:91], a[4:7], a[56:59], a[88:91]\n"
    "ds_read_b64_tr_b16 a[76:77], v55 offset:16384\n"
    "v_mfma_f32_16x16x32_bf16 a[92:95], a[4:7], a[60:63], a[92:95]\n"
    "ds_read_b64_tr_b16 a[78:79], v55 offset:17408\n"
    "v_mfma_f32_16x16x32_bf16 v[112:115], a[56:59], a[8:11], v[112:115]\n"
    "ds_read_b64_tr_b16 a[80:81], v56 offset:16384\n"
    "v_mfma_f32_16x16x32_bf16 v[128:131], a[60:63], a[8:11], v[128:131]\n"
    "buffer_load_dwordx4 v62, s[20:23], s82 offen offset:1024 lds\n"
    "v_mfma_f32_16x16x32_bf16 v[116:119], a[56:59], a[12:15], v[116:119]\n"
    "ds_read_b64_tr_b16 a[82:83], v56 offset:17408\n"
    "v_mfma_f32_16x16x32_bf16 v[132:135], a[60:63], a[12:15], v[132:135]\n"
    "ds_read_b64_tr_b16 a[84:85], v57 offset:16384\n"
    "v_mfma_f32_16x16x32_bf16 v[120:123], a[56:59], a[16:19], v[120:123]\n"
    "ds_read_b64_tr_b16 a[86:87], v57 offset:17408\n"
    "v_mfma_f32_16x16x32_bf16 v[136:139], a[60:63], a[16:19], v[136:139]\n"
    "ds_write_b32 v71, a104 offset:256\n"
    "v_mfma_f32_16x16x32_bf16 v[124:127], a[56:59], a[20:23], v[124:127]\n"
    "buffer_load_dwordx4 v63, s[20:23], s82 offen offset:2048 lds\n"
    "v_mfma_f32_16x16x32_bf16 v[140:143], a[60:63], a[20:23], v[140:143]\n"
    "s_waitcnt lgkmcnt(15)\n"
    "ds_write_b32 v71, a108 offset:320\n"
    "v_mfma_f32_16x16x32_bf16 a[96:99], a[4:7], a[64:67], a[96:99]\n"
    "ds_write_b32 v71, a88 offset:0\n"
    "v_mfma_f32_16x16x32_bf16 a[100:103], a[4:7], a[68:71], a[100:103]\n"
    "ds_write_b32 v71, a92 offset:64\n"
    "v_mfma_f32_16x16x32_bf16 v[144:147], a[64:67], a[8:11], v[144:147]\n"
    "v_mfma_f32_16x16x32_bf16 v[160:163], a[68:71], a[8:11], v[160:163]\n"
    "buffer_load_dwordx4 v64, s[20:23], s82 offen offset:3072 lds\n"
    "v_mfma_f32_16x16x32_bf16 v[148:151], a[64:67], a[12:15], v[148:151]\n"
    "v_mfma_f32_16x16x32_bf16 v[164:167], a[68:71], a[12:15], v[164:167]\n"
    "ds_write_b32 v71, a96 offset:128\n"
    "v_mfma_f32_16x16x32_bf16 v[152:155], a[64:67], a[16:19], v[152:155]\n"
    "ds_write_b32 v71, a100 offset:192\n"
    "v_mfma_f32_16x16x32_bf16 v[168:171], a[68:71], a[16:19], v[168:171]\n"
    "v_mfma_f32_16x16x32_bf16 v[156:159], a[64:67], a[20:23], v[156:159]\n"
    "v_mfma_f32_16x16x32_bf16 v[172:175], a[68:71], a[20:23], v[172:175]\n"
    "s_waitcnt lgkmcnt(0)\n"
    "s_barrier\n"
    "v_mfma_f32_16x16x32_bf16 v[112:115], a[72:75], a[24:27], v[112:115]\n"
    "s_add_u32 s82, s83, 163840\n"
    "ds_read_b128 v[10:13], v76 offset:0\n"
    "s_add_u32 m0, s73, 24576\n"
    "v_mfma_f32_16x16x32_bf16 v[128:131], a[76:79], a[24:27], v[128:131]\n"
    "buffer_load_dwordx4 v61, s[20:23], s82 offen offset:0 lds\n"
    "v_mfma_f32_16x16x32_bf16 v[116:119], a[72:75], a[28:31], v[116:119]\n"
    "s_lshl_b32 s70, s65, 1\n"
    "ds_read_b128 v[32:35], v76 offset:1024\n"
    "s_add_u32 s70, s70, s63\n"
    "v_mfma_f32_16x16x32_bf16 v[132:135], a[76:79], a[28:31], v[132:135]\n"
    "s_lshl_b32 s81, s66, 5\n"
    "ds_read_b128 v[14:17], v76 offset:64\n"
    "s_add_u32 s81, s81, s70\n"
    "v_mfma_f32_16x16x32_bf16 v[120:123], a[72:75], a[32:35], v[120:123]\n"
    "s_lshl_b32 s64, s81, 12\n"
    "ds_read_b128 v[36:39], v76 offset:1088\n"
    "s_add_u32 s64, s64, 7786496\n"
    "v_mfma_f32_16x16x32_bf16 v[136:139], a[76:79], a[32:35], v[136:139]\n"
    "s_lshl_b32 s81, s81, 3\n"
    "ds_read_b128 v[18:21], v76 offset:128\n"
    "s_add_u32 s78, s81, 36864\n"
    "v_mfma_f32_16x16x32_bf16 v[124:127], a[72:75], a[36:39], v[124:127]\n"
    "buffer_load_dwordx4 v62, s[20:23], s82 offen offset:1024 lds\n"
    "v_mfma_f32_16x16x32_bf16 v[140:143], a[76:79], a[36:39], v[140:143]\n"
    "s_add_u32 s81, s81, 45056\n"
    "ds_read_b128 v[40:43], v76 offset:1152\n"
    "s_mul_i32 s55, s66, 28\n"
    "v_mfma_f32_16x16x32_bf16 v[144:147], a[80:83], a[24:27], v[144:147]\n"
    "s_add_u32 s55, s55, s70\n"
    "ds_read_b128 v[24:27], v76 offset:192\n"
    "s_lshl_b32 s55, s55, 13\n"
    "v_mfma_f32_16x16x32_bf16 v[160:163], a[84:87], a[24:27], v[160:163]\n"
    "s_add_u32 s55, s55, 446464\n"
    "ds_read_b128 v[44:47], v76 offset:1216\n"
    "v_mov_b32 v56, s56\n"
    "v_mfma_f32_16x16x32_bf16 v[148:151], a[80:83], a[28:31], v[148:151]\n"
    "ds_read_b64 v[48:49], v70 offset:0\n"
    "v_mov_b32 v57, s57\n"
    "v_mfma_f32_16x16x32_bf16 v[164:167], a[84:87], a[28:31], v[164:167]\n"
    "s_waitcnt lgkmcnt(7)\n"
    "ds_read_b64 v[50:51], v70 offset:32\n"
    "v_add_f32 v10, v10, v32\n"
    "v_mfma_f32_16x16x32_bf16 v[152:155], a[80:83], a[32:35], v[152:155]\n"
    "buffer_load_dwordx4 v63, s[20:23], s82 offen offset:2048 lds\n"
    "v_mfma_f32_16x16x32_bf16 v[168:171], a[84:87], a[32:35], v[168:171]\n"
    "ds_read_b64 v[52:53], v70 offset:64\n"
    "v_add_f32 v11, v11, v33\n"
    "v_mfma_f32_16x16x32_bf16 v[156:159], a[80:83], a[36:39], v[156:159]\n"
    "ds_read_b64 v[54:55], v70 offset:96\n"
    "v_add_f32 v12, v12, v34\n"
    "v_mfma_f32_16x16x32_bf16 v[172:175], a[84:87], a[36:39], v[172:175]\n"
    "v_add_f32 v13, v13, v35\n"
    "s_waitcnt lgkmcnt(8)\n"
    "v_mfma_f32_16x16x32_bf16 v[28:31], a[24:27], a[48:51], v[28:31]\n"
    "v_add_f32 v14, v14, v36\n"
    "v_add_f32 v15, v15, v37\n"
    "v_mfma_f32_16x16x32_bf16 v[96:99], a[24:27], a[52:55], v[96:99]\n"
    "buffer_load_dwordx4 v64, s[20:23], s82 offen offset:3072 lds\n"
    "v_mfma_f32_16x16x32_bf16 v[72:75], a[28:31], a[48:51], v[72:75]\n"
    "v_add_f32 v16, v16, v38\n"
    "v_add_f32 v17, v17, v39\n"
    "v_mfma_f32_16x16x32_bf16 v[100:103], a[28:31], a[52:55], v[100:103]\n"
    "s_waitcnt lgkmcnt(6)\n"
    "v_add_f32 v18, v18, v40\n"
    "v_add_f32 v19, v19, v41\n"
    "v_mfma_f32_16x16x32_bf16 v[88:91], a[32:35], a[48:51], v[88:91]\n"
    "v_add_f32 v20, v20, v42\n"
    "v_add_f32 v21, v21, v43\n"
    "v_mfma_f32_16x16x32_bf16 v[104:107], a[32:35], a[52:55], v[104:107]\n"
    "s_waitcnt lgkmcnt(4)\n"
    "v_add_f32 v24, v24, v44\n"
    "v_add_f32 v25, v25, v45\n"
    "v_mfma_f32_16x16x32_bf16 v[92:95], a[36:39], a[48:51], v[92:95]\n"
    "v_add_f32 v26, v26, v46\n"
    "v_add_f32 v27, v27, v47\n"
    "v_mfma_f32_16x16x32_bf16 v[108:111], a[36:39], a[52:55], v[108:111]\n"
    "s_cmp_ge_u32 s65, 14\n"
    "s_cselect_b32 s55, 0x7f000000, s55\n"
    "s_waitcnt lgkmcnt(0)\n"
    "v_pk_fma_f32 v[220:221], v[10:11], v[48:49], v[188:189] op_sel:[0,1,0] op_sel_hi:[1,1,1] neg_lo:[0,1,0] neg_hi:[0,1,0]\n"
    "v_pk_fma_f32 v[222:223], v[12:13], v[48:49], v[190:191] op_sel:[0,1,0] op_sel_hi:[1,1,1] neg_lo:[0,1,0] neg_hi:[0,1,0]\n"
    "v_pk_fma_f32 v[220:221], v[112:113], v[48:49], v[220:221] op_sel:[0,0,0] op_sel_hi:[1,0,1]\n"
    "v_pk_fma_f32 v[222:223], v[114:115], v[48:49], v[222:223] op_sel:[0,0,0] op_sel_hi:[1,0,1]\n"
    "ds_read_b32 v32, v77 offset:256\n"
    "v_max_f32 v220, 0, v220\n"
    "v_max_f32 v221, 0, v221\n"
    "v_max_f32 v222, 0, v222\n"
    "v_max_f32 v223, 0, v223\n"
    "v_pk_mul_f32 v[220:221], v[220:221], v[220:221]\n"
    "v_pk_mul_f32 v[222:223], v[222:223], v[222:223]\n"
    "v_cvt_pk_bf16_f32 v228, v220, v221\n"
    "v_cvt_pk_bf16_f32 v229, v222, v223\n"
    "v_pk_fma_f32 v[224:225], v[14:15], v[48:49], v[192:193] op_sel:[0,1,0] op_sel_hi:[1,1,1] neg_lo:[0,1,0] neg_hi:[0,1,0]\n"
    "v_pk_fma_f32 v[226:227], v[16:17], v[48:49], v[194:195] op_sel:[0,1,0] op_sel_hi:[1,1,1] neg_lo:[0,1,0] neg_hi:[0,1,0]\n"
    "v_pk_fma_f32 v[224:225], v[128:129], v[48:49], v[224:225] op_sel:[0,0,0] op_sel_hi:[1,0,1]\n"
    "v_pk_fma_f32 v[226:227], v[130:131], v[48:49], v[226:227] op_sel:[0,0,0] op_sel_hi:[1,0,1]\n"
    "v_max_f32 v224, 0, v224\n"
    "v_max_f32 v225, 0, v225\n"
    "v_max_f32 v226, 0, v226\n"
    "ds_read_b32 v33, v77 offset:1280\n"
    "v_max_f32 v227, 0, v227\n"
    "v_pk_mul_f32 v[224:225], v[224:225], v[224:225]\n"
    "v_pk_mul_f32 v[226:227], v[226:227], v[226:227]\n"
    "v_cvt_pk_bf16_f32 v230, v224, v225\n"
    "v_cvt_pk_bf16_f32 v231, v226, v227\n"
    "buffer_store_dwordx4 v[228:231], v3, s[16:19], s55 offen offset:0 sc1\n"
    "v_pk_fma_f32 v[220:221], v[10:11], v[50:51], v[188:189] op_sel:[0,1,0] op_sel_hi:[1,1,1] neg_lo:[0,1,0] neg_hi:[0,1,0]\n"
    "v_pk_fma_f32 v[222:223], v[12:13], v[50:51], v[190:191] op_sel:[0,1,0] op_sel_hi:[1,1,1] neg_lo:[0,1,0] neg_hi:[0,1,0]\n"
    "v_pk_fma_f32 v[220:221], v[116:117], v[50:51], v[220:221] op_sel:[0,0,0] op_sel_hi:[1,0,1]\n"
    "v_pk_fma_f32 v[222:223], v[118:119], v[50:51], v[222:223] op_sel:[0,0,0] op_sel_hi:[1,0,1]\n"
    "v_max_f32 v220, 0, v220\n"
    "v_max_f32 v221, 0, v221\n"
    "v_max_f32 v222, 0, v222\n"
    "v_max_f32 v223, 0, v223\n"
    "v_pk_mul_f32 v[220:221], v[220:221], v[220:221]\n"
    "ds_read_b32 v36, v77 offset:320\n"
    "v_pk_mul_f32 v[222:223], v[222:223], v[222:223]\n"
    "v_cvt_pk_bf16_f32 v232, v220, v221\n"
    "v_cvt_pk_bf16_f32 v233, v222, v223\n"
    "v_pk_fma_f32 v[224:225], v[14:15], v[50:51], v[192:193] op_sel:[0,1,0] op_sel_hi:[1,1,1] neg_lo:[0,1,0] neg_hi:[0,1,0]\n"
    "v_pk_fma_f32 v[226:227], v[16:17], v[50:51], v[194:195] op_sel:[0,1,0] op_sel_hi:[1,1,1] neg_lo:[0,1,0] neg_hi:[0,1,0]\n"
    "v_pk_fma_f32 v[224:225], v[132:133], v[50:51], v[224:225] op_sel:[0,0,0] op_sel_hi:[1,0,1]\n"
    "v_pk_fma_f32 v[226:227], v[134:135], v[50:51], v[226:227] op_sel:[0,0,0] op_sel_hi:[1,0,1]\n"
    "v_max_f32 v224, 0, v224\n"
    "v_max_f32 v225, 0, v225\n"
    "v_max_f32 v226, 0, v226\n"
    "v_max_f32 v227, 0, v227\n"
    "v_pk_mul_f32 v[224:225], v[224:225], v[224:225]\n"
    "v_pk_mul_f32 v[226:227], v[226:227], v[226:227]\n"
    "v_cvt_pk_bf16_f32 v234, v224, v225\n"
    "v_cvt_pk_bf16_f32 v235, v226, v227\n"
    "ds_read_b32 v37, v77 offset:1344\n"
    "buffer_store_dwordx4 v[232:235], v3, s[16:19], s55 offen offset:1024 sc1\n"
    "v_pk_fma_f32 v[220:221], v[10:11], v[52:53], v[188:189] op_sel:[0,1,0] op_sel_hi:[1,1,1] neg_lo:[0,1,0] neg_hi:[0,1,0]\n"
    "v_pk_fma_f32 v[222:223], v[12:13], v[52:53], v[190:191] op_sel:[0,1,0] op_sel_hi:[1,1,1] neg_lo:[0,1,0] neg_hi:[0,1,0]\n"
    "v_pk_fma_f32 v[220:221], v[120:121], v[52:53], v[220:221] op_sel:[0,0,0] op_sel_hi:[1,0,1]\n"
    "v_pk_fma_f32 v[222:223], v[122:123], v[52:53], v[222:223] op_sel:[0,0,0] op_sel_hi:[1,0,1]\n"
    "v_max_f32 v220, 0, v220\n"
    "v_max_f32 v221, 0, v221\n"
    "v_max_f32 v222, 0, v222\n"
    "v_max_f32 v223, 0, v223\n"
    "v_pk_mul_f32 v[220:221], v[220:221], v[220:221]\n"
    "v_pk_mul_f32 v[222:223], v[222:223], v[222:223]\n"
    "v_cvt_pk_bf16_f32 v236, v220, v221\n"
    "v_cvt_pk_bf16_f32 v237, v222, v223\n"
    "v_pk_fma_f32 v[224:225], v[14:15], v[52:53], v[192:193] op_sel:[0,1,0] op_sel_hi:[1,1,1] neg_lo:[0,1,0] neg_hi:[0,1,0]\n"
    "v_pk_fma_f32 v[226:227], v[16:17], v[52:53], v[194:195] op_sel:[0,1,0] op_sel_hi:[1,1,1] neg_lo:[0,1,0] neg_hi:[0,1,0]\n"
    "ds_read_b128 v[40:43], v23 offset:0\n"
    "v_pk_fma_f32 v[224:225], v[136:137], v[52:53], v[224:225] op_sel:[0,0,0] op_sel_hi:[1,0,1]\n"
    "v_pk_fma_f32 v[226:227], v[138:139], v[52:53], v[226:227] op_sel:[0,0,0] op_sel_hi:[1,0,1]\n"
    "v_max_f32 v224, 0, v224\n"
    "v_max_f32 v225, 0, v225\n"
    "v_max_f32 v226, 0, v226\n"
    "v_max_f32 v227, 0, v227\n"
    "v_pk_mul_f32 v[224:225], v[224:225], v[224:225]\n"
    "v_pk_mul_f32 v[226:227], v[226:227], v[226:227]\n"
    "v_cvt_pk_bf16_f32 v238, v224, v225\n"
    "v_cvt_pk_bf16_f32 v239, v226, v227\n"
    "buffer_store_dwordx4 v[236:239], v3, s[16:19], s55 offen offset:2048 sc1\n"
    "v_pk_fma_f32 v[220:221], v[10:11], v[54:55], v[188:189] op_sel:[0,1,0] op_sel_hi:[1,1,1] neg_lo:[0,1,0] neg_hi:[0,1,0]\n"
    "v_pk_fma_f32 v[222:223], v[12:13], v[54:55], v[190:191] op_sel:[0,1,0] op_sel_hi:[1,1,1] neg_lo:[0,1,0] neg_hi:[0,1,0]\n"
    "v_pk_fma_f32 v[220:221], v[124:125], v[54:55], v[220:221] op_sel:[0,0,0] op_sel_hi:[1,0,1]\n"
    "v_pk_fma_f32 v[222:223], v[126:127], v[54:55], v[222:223] op_sel:[0,0,0] op_sel_hi:[1,0,1]\n"
    "ds_read_b128 v[44:47], v23 offset:16\n"
    "v_max_f32 v220, 0, v220\n"
    "v_max_f32 v221, 0, v221\n"
    "v_max_f32 v222, 0, v222\n"
    "v_max_f32 v223, 0, v223\n"
    "v_pk_mul_f32 v[220:221], v[220:221], v[220:221]\n"
    "v_pk_mul_f32 v[222:223], v[222:223], v[222:223]\n"
    "v_cvt_pk_bf16_f32 v228, v220, v221\n"
    "v_cvt_pk_bf16_f32 v229, v222, v223\n"
    "v_pk_fma_f32 v[224:225], v[14:15], v[54:55], v[192:193] op_sel:[0,1,0] op_sel_hi:[1,1,1] neg_lo:[0,1,0] neg_hi:[0,1,0]\n"
    "v_pk_fma_f32 v[226:227], v[16:17], v[54:55], v[194:195] op_sel:[0,1,0] op_sel_hi:[1,1,1] neg_lo:[0,1,0] neg_hi:[0,1,0]\n"
    "v_pk_fma_f32 v[224:225], v[140:141], v[54:55], v[224:225] op_sel:[0,0,0] op_sel_hi:[1,0,1]\n"
    "v_pk_fma_f32 v[226:227], v[142:143], v[54:55], v[226:227] op_sel:[0,0,0] op_sel_hi:[1,0,1]\n"
    "v_max_f32 v224, 0, v224\n"
    "v_max_f32 v225, 0, v225\n"
    "v_max_f32 v226, 0, v226\n"
    "ds_read_b128 v[78:81], v23 offset:32\n"
    "v_max_f32 v227, 0, v227\n"
    "v_pk_mul_f32 v[224:225], v[224:225], v[224:225]\n"
    "v_pk_mul_f32 v[226:227], v[226:227], v[226:227]\n"
    "v_cvt_pk_bf16_f32 v230, v224, v225\n"
    "v_cvt_pk_bf16_f32 v231, v226, v227\n"
    "buffer_store_dwordx4 v[228:231], v3, s[16:19], s55 offen offset:3072 sc1\n"
    "s_add_u32 s55, s55, 4096\n"
    "v_pk_fma_f32 v[220:221], v[18:19], v[48:49], v[196:197] op_sel:[0,1,0] op_sel_hi:[1,1,1] neg_lo:[0,1,0] neg_hi:[0,1,0]\n"
    "v_pk_fma_f32 v[222:223], v[20:21], v[48:49], v[198:199] op_sel:[0,1,0] op_sel_hi:[1,1,1] neg_lo:[0,1,0] neg_hi:[0,1,0]\n"
    "v_pk_fma_f32 v[220:221], v[144:145], v[48:49], v[220:221] op_sel:[0,0,0] op_sel_hi:[1,0,1]\n"
    "v_pk_fma_f32 v[222:223], v[146:147], v[48:49], v[222:223] op_sel:[0,0,0] op_sel_hi:[1,0,1]\n"
    "v_max_f32 v220, 0, v220\n"
    "v_max_f32 v221, 0, v221\n"
    "v_max_f32 v222, 0, v222\n"
    "v_max_f32 v223, 0, v223\n"
    "ds_read_b128 v[82:85], v23 offset:48\n"
    "v_pk_mul_f32 v[220:221], v[220:221], v[220:221]\n"
    "v_pk_mul_f32 v[222:223], v[222:223], v[222:223]\n"
    "v_cvt_pk_bf16_f32 v232, v220, v221\n"
    "v_cvt_pk_bf16_f32 v233, v222, v223\n"
    "v_pk_fma_f32 v[224:225], v[24:25], v[48:49], v[200:201] op_sel:[0,1,0] op_sel_hi:[1,1,1] neg_lo:[0,1,0] neg_hi:[0,1,0]\n"
    "v_pk_fma_f32 v[226:227], v[26:27], v[48:49], v[202:203] op_sel:[0,1,0] op_sel_hi:[1,1,1] neg_lo:[0,1,0] neg_hi:[0,1,0]\n"
    "v_pk_fma_f32 v[224:225], v[160:161], v[48:49], v[224:225] op_sel:[0,0,0] op_sel_hi:[1,0,1]\n"
    "v_pk_fma_f32 v[226:227], v[162:163], v[48:49], v[226:227] op_sel:[0,0,0] op_sel_hi:[1,0,1]\n"
    "v_max_f32 v224, 0, v224\n"
    "v_max_f32 v225, 0, v225\n"
    "v_max_f32 v226, 0, v226\n"
    "v_max_f32 v227, 0, v227\n"
    "v_pk_mul_f32 v[224:225], v[224:225], v[224:225]\n"
    "v_pk_mul_f32 v[226:227], v[226:227], v[226:227]\n"
    "v_cvt_pk_bf16_f32 v234, v224, v225\n"
    "ds_read_b128 v[204:207], v23 offset:64\n"
    "v_cvt_pk_bf16_f32 v235, v226, v227\n"
    "buffer_store_dwordx4 v[232:235], v3, s[16:19], s55 offen offset:0 sc1\n"
    "v_pk_fma_f32 v[220:221], v[18:19], v[50:51], v[196:197] op_sel:[0,1,0] op_sel_hi:[1,1,1] neg_lo:[0,1,0] neg_hi:[0,1,0]\n"
    "v_pk_fma_f32 v[222:223], v[20:21], v[50:51], v[198:199] op_sel:[0,1,0] op_sel_hi:[1,1,1] neg_lo:[0,1,0] neg_hi:[0,1,0]\n"
    "v_pk_fma_f32 v[220:221], v[148:149], v[50:51], v[220:221] op_sel:[0,0,0] op_sel_hi:[1,0,1]\n"
    "v_pk_fma_f32 v[222:223], v[150:151], v[50:51], v[222:223] op_sel:[0,0,0] op_sel_hi:[1,0,1]\n"
    "v_max_f32 v220, 0, v220\n"
    "v_max_f32 v221, 0, v221\n"
    "v_max_f32 v222, 0, v222\n"
    "v_max_f32 v223, 0, v223\n"
    "v_pk_mul_f32 v[220:221], v[220:221], v[220:221]\n"
    "v_pk_mul_f32 v[222:223], v[222:223], v[222:223]\n"
    "v_cvt_pk_bf16_f32 v236, v220, v221\n"
    "v_cvt_pk_bf16_f32 v237, v222, v223\n"
    "v_pk_fma_f32 v[224:225], v[24:25], v[50:51], v[200:201] op_sel:[0,1,0] op_sel_hi:[1,1,1] neg_lo:[0,1,0] neg_hi:[0,1,0]\n"
    "ds_read_b128 v[208:211], v23 offset:80\n"
    "v_pk_fma_f32 v[226:227], v[26:27], v[50:51], v[202:203] op_sel:[0,1,0] op_sel_hi:[1,1,1] neg_lo:[0,1,0] neg_hi:[0,1,0]\n"
    "v_pk_fma_f32 v[224:225], v[164:165], v[50:51], v[224:225] op_sel:[0,0,0] op_sel_hi:[1,0,1]\n"
    "v_pk_fma_f32 v[226:227], v[166:167], v[50:51], v[226:227] op_sel:[0,0,0] op_sel_hi:[1,0,1]\n"
    "v_max_f32 v224, 0, v224\n"
    "v_max_f32 v225, 0, v225\n"
    "v_max_f32 v226, 0, v226\n"
    "v_max_f32 v227, 0, v227\n"
    "v_pk_mul_f32 v[224:225], v[224:225], v[224:225]\n"
    "v_pk_mul_f32 v[226:227], v[226:227], v[226:227]\n"
    "v_cvt_pk_bf16_f32 v238, v224, v225\n"
    "v_cvt_pk_bf16_f32 v239, v226, v227\n"
    "buffer_store_dwordx4 v[236:239], v3, s[16:19], s55 offen offset:1024 sc1\n"
    "v_pk_fma_f32 v[220:221], v[18:19], v[52:53], v[196:197] op_sel:[0,1,0] op_sel_hi:[1,1,1] neg_lo:[0,1,0] neg_hi:[0,1,0]\n"
    "v_pk_fma_f32 v[222:223], v[20:21], v[52:53], v[198:199] op_sel:[0,1,0] op_sel_hi:[1,1,1] neg_lo:[0,1,0] neg_hi:[0,1,0]\n"
    "v_pk_fma_f32 v[220:221], v[152:153], v[52:53], v[220:221] op_sel:[0,0,0] op_sel_hi:[1,0,1]\n"
    "ds_read_b128 v[212:215], v23 offset:96\n"
    "v_pk_fma_f32 v[222:223], v[154:155], v[52:53], v[222:223] op_sel:[0,0,0] op_sel_hi:[1,0,1]\n"
    "v_max_f32 v220, 0, v220\n"
    "v_max_f32 v221, 0, v221\n"
    "v_max_f32 v222, 0, v222\n"
    "v_max_f32 v223, 0, v223\n"
    "v_pk_mul_f32 v[220:221], v[220:221], v[220:221]\n"
    "v_pk_mul_f32 v[222:223], v[222:223], v[222:223]\n"
    "v_cvt_pk_bf16_f32 v228, v220, v221\n"
    "v_cvt_pk_bf16_f32 v229, v222, v223\n"
    "v_pk_fma_f32 v[224:225], v[24:25], v[52:53], v[200:201] op_sel:[0,1,0] op_sel_hi:[1,1,1] neg_lo:[0,1,0] neg_hi:[0,1,0]\n"
    "v_pk_fma_f32 v[226:227], v[26:27], v[52:53], v[202:203] op_sel:[0,1,0] op_sel_hi:[1,1,1] neg_lo:[0,1,0] neg_hi:[0,1,0]\n"
    "v_pk_fma_f32 v[224:225], v[168:169], v[52:53], v[224:225] op_sel:[0,0,0] op_sel_hi:[1,0,1]\n"
    "v_pk_fma_f32 v[226:227], v[170:171], v[52:53], v[226:227] op_sel:[0,0,0] op_sel_hi:[1,0,1]\n"
    "v_max_f32 v224, 0, v224\n"
    "v_max_f32 v225, 0, v225\n"
    "ds_read_b128 v[216:219], v23 offset:112\n"
    "v_max_f32 v226, 0, v226\n"
    "v_max_f32 v227, 0, v227\n"
    "v_pk_mul_f32 v[224:225], v[224:225], v[224:225]\n"
    "v_pk_mul_f32 v[226:227], v[226:227], v[226:227]\n"
    "v_cvt_pk_bf16_f32 v230, v224, v225\n"
    "v_cvt_pk_bf16_f32 v231, v226, v227\n"
    "buffer_store_dwordx4 v[228:231], v3, s[16:19], s55 offen offset:2048 sc1\n"
    "v_pk_fma_f32 v[220:221], v[18:19], v[54:55], v[196:197] op_sel:[0,1,0] op_sel_hi:[1,1,1] neg_lo:[0,1,0] neg_hi:[0,1,0]\n"
    "v_pk_fma_f32 v[222:223], v[20:21], v[54:55], v[198:199] op_sel:[0,1,0] op_sel_hi:[1,1,1] neg_lo:[0,1,0] neg_hi:[0,1,0]\n"
    "v_pk_fma_f32 v[220:221], v[156:157], v[54:55], v[220:221] op_sel:[0,0,0] op_sel_hi:[1,0,1]\n"
    "v_pk_fma_f32 v[222:223], v[158:159], v[54:55], v[222:223] op_sel:[0,0,0] op_sel_hi:[1,0,1]\n"
    "v_max_f32 v220, 0, v220\n"
    "v_max_f32 v221, 0, v221\n"
    "v_max_f32 v222, 0, v222\n"
    "v_max_f32 v223, 0, v223\n"
    "v_pk_mul_f32 v[220:221], v[220:221], v[220:221]\n"
    "v_pk_mul_f32 v[222:223], v[222:223], v[222:223]\n"
    "v_cvt_pk_bf16_f32 v232, v220, v221\n"
    "v_cvt_pk_bf16_f32 v233, v222, v223\n"
    "v_pk_fma_f32 v[224:225], v[24:25], v[54:55], v[200:201] op_sel:[0,1,0] op_sel_hi:[1,1,1] neg_lo:[0,1,0] neg_hi:[0,1,0]\n"
    "v_pk_fma_f32 v[226:227], v[26:27], v[54:55], v[202:203] op_sel:[0,1,0] op_sel_hi:[1,1,1] neg_lo:[0,1,0] neg_hi:[0,1,0]\n"
    "v_pk_fma_f32 v[224:225], v[172:173], v[54:55], v[224:225] op_sel:[0,0,0] op_sel_hi:[1,0,1]\n"
    "v_pk_fma_f32 v[226:227], v[174:175], v[54:55], v[226:227] op_sel:[0,0,0] op_sel_hi:[1,0,1]\n"
    "v_max_f32 v224, 0, v224\n"
    "v_max_f32 v225, 0, v225\n"
    "v_max_f32 v226, 0, v226\n"
    "v_max_f32 v227, 0, v227\n"
    "v_pk_mul_f32 v[224:225], v[224:225], v[224:225]\n"
    "v_pk_mul_f32 v[226:227], v[226:227], v[226:227]\n"
    "v_cvt_pk_bf16_f32 v234, v224, v225\n"
    "v_cvt_pk_bf16_f32 v235, v226, v227\n"
    "buffer_store_dwordx4 v[232:235], v3, s[16:19], s55 offen offset:3072 sc1\n"
    "v_pk_mul_f32 v[12:13], v[96:97], v[186:187] op_sel:[0,0] op_sel_hi:[0,1]\n"
    "v_pk_mul_f32 v[14:15], v[96:97], v[186:187] op_sel:[1,0] op_sel_hi:[1,1]\n"
    "v_pk_mul_f32 v[16:17], v[98:99], v[186:187] op_sel:[0,0] op_sel_hi:[0,1]\n"
    "v_pk_mul_f32 v[18:19], v[98:99], v[186:187] op_sel:[1,0] op_sel_hi:[1,1]\n"
    "v_pk_mul_f32 v[20:21], v[100:101], v[186:187] op_sel:[0,0] op_sel_hi:[0,1]\n"
    "v_pk_mul_f32 v[22:23], v[100:101], v[186:187] op_sel:[1,0] op_sel_hi:[1,1]\n"
    "v_pk_mul_f32 v[24:25], v[102:103], v[186:187] op_sel:[0,0] op_sel_hi:[0,1]\n"
    "v_pk_mul_f32 v[26:27], v[102:103], v[186:187] op_sel:[1,0] op_sel_hi:[1,1]\n"
    "v_pk_mul_f32 v[48:49], v[104:105], v[186:187] op_sel:[0,0] op_sel_hi:[0,1]\n"
    "v_pk_mul_f32 v[50:51], v[104:105], v[186:187] op_sel:[1,0] op_sel_hi:[1,1]\n"
    "s_add_u32 s82, s83, 196608\n"
    "s_add_u32 m0, s73, 90112\n"
    "s_nop 0\n"
    "buffer_load_dwordx4 v61, s[20:23], s82 offen offset:0 lds\n"
    "v_pk_mul_f32 v[52:53], v[106:107], v[186:187] op_sel:[0,0] op_sel_hi:[0,1]\n"
    "v_pk_mul_f32 v[54:55], v[106:107], v[186:187] op_sel:[1,0] op_sel_hi:[1,1]\n"
    "v_pk_mul_f32 v[112:113], v[108:109], v[186:187] op_sel:[0,0] op_sel_hi:[0,1]\n"
    "v_pk_mul_f32 v[114:115], v[108:109], v[186:187] op_sel:[1,0] op_sel_hi:[1,1]\n"
    "v_pk_mul_f32 v[116:117], v[110:111], v[186:187] op_sel:[0,0] op_sel_hi:[0,1]\n"
    "v_pk_mul_f32 v[118:119], v[110:111], v[186:187] op_sel:[1,0] op_sel_hi:[1,1]\n"
    "v_pk_fma_f32 v[12:13], v[28:29], v[184:185], v[12:13] op_sel:[0,0,0] op_sel_hi:[0,1,1]\n"
    "v_pk_fma_f32 v[14:15], v[28:29], v[184:185], v[14:15] op_sel:[1,0,0] op_sel_hi:[1,1,1]\n"
    "v_pk_fma_f32 v[16:17], v[30:31], v[184:185], v[16:17] op_sel:[0,0,0] op_sel_hi:[0,1,1]\n"
    "v_pk_fma_f32 v[18:19], v[30:31], v[184:185], v[18:19] op_sel:[1,0,0] op_sel_hi:[1,1,1]\n"
    "v_pk_fma_f32 v[20:21], v[72:73], v[184:185], v[20:21] op_sel:[0,0,0] op_sel_hi:[0,1,1]\n"
    "v_pk_fma_f32 v[22:23], v[72:73], v[184:185], v[22:23] op_sel:[1,0,0] op_sel_hi:[1,1,1]\n"
    "v_pk_fma_f32 v[24:25], v[74:75], v[184:185], v[24:25] op_sel:[0,0,0] op_sel_hi:[0,1,1]\n"
    "v_pk_fma_f32 v[26:27], v[74:75], v[184:185], v[26:27] op_sel:[1,0,0] op_sel_hi:[1,1,1]\n"
    "v_pk_fma_f32 v[48:49], v[88:89], v[184:185], v[48:49] op_sel:[0,0,0] op_sel_hi:[0,1,1]\n"
    "v_pk_fma_f32 v[50:51], v[88:89], v[184:185], v[50:51] op_sel:[1,0,0] op_sel_hi:[1,1,1]\n"
    "v_pk_fma_f32 v[52:53], v[90:91], v[184:185], v[52:53] op_sel:[0,0,0] op_sel_hi:[0,1,1]\n"
    "v_pk_fma_f32 v[54:55], v[90:91], v[184:185], v[54:55] op_sel:[1,0,0] op_sel_hi:[1,1,1]\n"
    "v_pk_fma_f32 v[112:113], v[92:93], v[184:185], v[112:113] op_sel:[0,0,0] op_sel_hi:[0,1,1]\n"
    "v_pk_fma_f32 v[114:115], v[92:93], v[184:185], v[114:115] op_sel:[1,0,0] op_sel_hi:[1,1,1]\n"
    "v_pk_fma_f32 v[116:117], v[94:95], v[184:185], v[116:117] op_sel:[0,0,0] op_sel_hi:[0,1,1]\n"
    "v_pk_fma_f32 v[118:119], v[94:95], v[184:185], v[118:119] op_sel:[1,0,0] op_sel_hi:[1,1,1]\n"
    "s_waitcnt lgkmcnt(0)\n"
    "v_add_f32 v32, v32, v33\n"
    "v_add_f32 v36, v36, v37\n"
    "v_pk_mul_f32 v[10:11], v[36:37], v[186:187] op_sel:[0,0] op_sel_hi:[0,1]\n"
    "v_pk_fma_f32 v[10:11], v[32:33], v[184:185], v[10:11] op_sel:[0,0,0] op_sel_hi:[0,1,1]\n"
    "v_pk_mul_f32 v[34:35], v[10:11], v[40:41] op_sel:[0,1] op_sel_hi:[1,1] neg_lo:[0,1] neg_hi:[0,1]\n"
    "v_pk_fma_f32 v[12:13], v[12:13], v[40:41], v[34:35] op_sel:[0,0,0] op_sel_hi:[1,0,1]\n"
    "v_pk_mul_f32 v[38:39], v[10:11], v[42:43] op_sel:[0,1] op_sel_hi:[1,1] neg_lo:[0,1] neg_hi:[0,1]\n"
    "v_pk_fma_f32 v[14:15], v[14:15], v[42:43], v[38:39] op_sel:[0,0,0] op_sel_hi:[1,0,1]\n"
    "v_pk_mul_f32 v[34:35], v[10:11], v[44:45] op_sel:[0,1] op_sel_hi:[1,1] neg_lo:[0,1] neg_hi:[0,1]\n"
    "buffer_load_dwordx4 v62, s[20:23], s82 offen offset:1024 lds\n"
    "v_pk_fma_f32 v[76:77], v[12:13], v[86:87], v[14:15] op_sel:[0,0,0] op_sel_hi:[1,0,1]\n"
    "v_pk_fma_f32 v[16:17], v[16:17], v[44:45], v[34:35] op_sel:[0,0,0] op_sel_hi:[1,0,1]\n"
    "v_pk_fma_f32 v[70:71], v[12:13], v[86:87], v[76:77] op_sel:[1,1,0] op_sel_hi:[0,1,1] neg_lo:[0,1,0]\n"
    "v_pk_mul_f32 v[38:39], v[10:11], v[46:47] op_sel:[0,1] op_sel_hi:[1,1] neg_lo:[0,1] neg_hi:[0,1]\n"
    "v_pk_fma_f32 v[76:77], v[70:71], v[86:87], v[16:17] op_sel:[0,0,0] op_sel_hi:[1,0,1]\n"
    "v_pk_fma_f32 v[18:19], v[18:19], v[46:47], v[38:39] op_sel:[0,0,0] op_sel_hi:[1,0,1]\n"
    "v_pk_fma_f32 v[70:71], v[70:71], v[86:87], v[76:77] op_sel:[1,1,0] op_sel_hi:[0,1,1] neg_lo:[0,1,0]\n"
    "v_pk_mul_f32 v[34:35], v[10:11], v[78:79] op_sel:[0,1] op_sel_hi:[1,1] neg_lo:[0,1] neg_hi:[0,1]\n"
    "v_pk_fma_f32 v[76:77], v[70:71], v[86:87], v[18:19] op_sel:[0,0,0] op_sel_hi:[1,0,1]\n"
    "v_pk_fma_f32 v[20:21], v[20:21], v[78:79], v[34:35] op_sel:[0,0,0] op_sel_hi:[1,0,1]\n"
    "v_pk_fma_f32 v[70:71], v[70:71], v[86:87], v[76:77] op_sel:[1,1,0] op_sel_hi:[0,1,1] neg_lo:[0,1,0]\n"
    "v_pk_mul_f32 v[38:39], v[10:11], v[80:81] op_sel:[0,1] op_sel_hi:[1,1] neg_lo:[0,1] neg_hi:[0,1]\n"
    "v_pk_fma_f32 v[76:77], v[70:71], v[86:87], v[20:21] op_sel:[0,0,0] op_sel_hi:[1,0,1]\n"
    "v_pk_fma_f32 v[22:23], v[22:23], v[80:81], v[38:39] op_sel:[0,0,0] op_sel_hi:[1,0,1]\n"
    "v_pk_fma_f32 v[70:71], v[70:71], v[86:87], v[76:77] op_sel:[1,1,0] op_sel_hi:[0,1,1] neg_lo:[0,1,0]\n"
    "v_pk_mul_f32 v[34:35], v[10:11], v[82:83] op_sel:[0,1] op_sel_hi:[1,1] neg_lo:[0,1] neg_hi:[0,1]\n"
    "v_pk_fma_f32 v[76:77], v[70:71], v[86:87], v[22:23] op_sel:[0,0,0] op_sel_hi:[1,0,1]\n"
    "v_pk_fma_f32 v[24:25], v[24:25], v[82:83], v[34:35] op_sel:[0,0,0] op_sel_hi:[1,0,1]\n"
    "v_pk_fma_f32 v[70:71], v[70:71], v[86:87], v[76:77] op_sel:[1,1,0] op_sel_hi:[0,1,1] neg_lo:[0,1,0]\n"
    "v_pk_mul_f32 v[38:39], v[10:11], v[84:85] op_sel:[0,1] op_sel_hi:[1,1] neg_lo:[0,1] neg_hi:[0,1]\n"
    "v_pk_fma_f32 v[76:77], v[70:71], v[86:87], v[24:25] op_sel:[0,0,0] op_sel_hi:[1,0,1]\n"
    "v_pk_fma_f32 v[26:27], v[26:27], v[84:85], v[38:39] op_sel:[0,0,0] op_sel_hi:[1,0,1]\n"
    "v_pk_fma_f32 v[70:71], v[70:71], v[86:87], v[76:77] op_sel:[1,1,0] op_sel_hi:[0,1,1] neg_lo:[0,1,0]\n"
    "v_pk_mul_f32 v[34:35], v[10:11], v[204:205] op_sel:[0,1] op_sel_hi:[1,1] neg_lo:[0,1] neg_hi:[0,1]\n"
    "v_pk_fma_f32 v[76:77], v[70:71], v[86:87], v[26:27] op_sel:[0,0,0] op_sel_hi:[1,0,1]\n"
    "v_pk_fma_f32 v[48:49], v[48:49], v[204:205], v[34:35] op_sel:[0,0,0] op_sel_hi:[1,0,1]\n"
    "v_pk_fma_f32 v[70:71], v[70:71], v[86:87], v[76:77] op_sel:[1,1,0] op_sel_hi:[0,1,1] neg_lo:[0,1,0]\n"
    "v_pk_mul_f32 v[38:39], v[10:11], v[206:207] op_sel:[0,1] op_sel_hi:[1,1] neg_lo:[0,1] neg_hi:[0,1]\n"
    "v_pk_fma_f32 v[76:77], v[70:71], v[86:87], v[48:49] op_sel:[0,0,0] op_sel_hi:[1,0,1]\n"
    "v_pk_fma_f32 v[50:51], v[50:51], v[206:207], v[38:39] op_sel:[0,0,0] op_sel_hi:[1,0,1]\n"
    "v_pk_fma_f32 v[70:71], v[70:71], v[86:87], v[76:77] op_sel:[1,1,0] op_sel_hi:[0,1,1] neg_lo:[0,1,0]\n"
    "v_pk_mul_f32 v[34:35], v[10:11], v[208:209] op_sel:[0,1] op_sel_hi:[1,1] neg_lo:[0,1] neg_hi:[0,1]\n"
    "buffer_load_dwordx4 v63, s[20:23], s82 offen offset:2048 lds\n"
    "v_pk_fma_f32 v[76:77], v[70:71], v[86:87], v[50:51] op_sel:[0,0,0] op_sel_hi:[1,0,1]\n"
    "v_pk_fma_f32 v[52:53], v[52:53], v[208:209], v[34:35] op_sel:[0,0,0] op_sel_hi:[1,0,1]\n"
    "v_pk_fma_f32 v[70:71], v[70:71], v[86:87], v[76:77] op_sel:[1,1,0] op_sel_hi:[0,1,1] neg_lo:[0,1,0]\n"
    "v_pk_mul_f32 v[38:39], v[10:11], v[210:211] op_sel:[0,1] op_sel_hi:[1,1] neg_lo:[0,1] neg_hi:[0,1]\n"
    "v_pk_fma_f32 v[76:77], v[70:71], v[86:87], v[52:53] op_sel:[0,0,0] op_sel_hi:[1,0,1]\n"
    "v_pk_fma_f32 v[54:55], v[54:55], v[210:211], v[38:39] op_sel:[0,0,0] op_sel_hi:[1,0,1]\n"
    "v_pk_fma_f32 v[70:71], v[70:71], v[86:87], v[76:77] op_sel:[1,1,0] op_sel_hi:[0,1,1] neg_lo:[0,1,0]\n"
    "v_pk_mul_f32 v[34:35], v[10:11], v[212:213] op_sel:[0,1] op_sel_hi:[1,1] neg_lo:[0,1] neg_hi:[0,1]\n"
    "v_pk_fma_f32 v[76:77], v[70:71], v[86:87], v[54:55] op_sel:[0,0,0] op_sel_hi:[1,0,1]\n"
    "v_pk_fma_f32 v[112:113], v[112:113], v[212:213], v[34:35] op_sel:[0,0,0] op_sel_hi:[1,0,1]\n"
    "v_pk_fma_f32 v[70:71], v[70:71], v[86:87], v[76:77] op_sel:[1,1,0] op_sel_hi:[0,1,1] neg_lo:[0,1,0]\n"
    "v_pk_mul_f32 v[38:39], v[10:11], v[214:215] op_sel:[0,1] op_sel_hi:[1,1] neg_lo:[0,1] neg_hi:[0,1]\n"
    "v_pk_fma_f32 v[76:77], v[70:71], v[86:87], v[112:113] op_sel:[0,0,0] op_sel_hi:[1,0,1]\n"
    "v_pk_fma_f32 v[114:115], v[114:115], v[214:215], v[38:39] op_sel:[0,0,0] op_sel_hi:[1,0,1]\n"
    "v_pk_fma_f32 v[70:71], v[70:71], v[86:87], v[76:77] op_sel:[1,1,0] op_sel_hi:[0,1,1] neg_lo:[0,1,0]\n"
    "v_pk_mul_f32 v[34:35], v[10:11], v[216:217] op_sel:[0,1] op_sel_hi:[1,1] neg_lo:[0,1] neg_hi:[0,1]\n"
    "v_pk_fma_f32 v[76:77], v[70:71], v[86:87], v[114:115] op_sel:[0,0,0] op_sel_hi:[1,0,1]\n"
    "v_pk_fma_f32 v[116:117], v[116:117], v[216:217], v[34:35] op_sel:[0,0,0] op_sel_hi:[1,0,1]\n"
    "v_pk_fma_f32 v[70:71], v[70:71], v[86:87], v[76:77] op_sel:[1,1,0] op_sel_hi:[0,1,1] neg_lo:[0,1,0]\n"
    "v_pk_mul_f32 v[38:39], v[10:11], v[218:219] op_sel:[0,1] op_sel_hi:[1,1] neg_lo:[0,1] neg_hi:[0,1]\n"
    "v_pk_fma_f32 v[76:77], v[70:71], v[86:87], v[116:117] op_sel:[0,0,0] op_sel_hi:[1,0,1]\n"
    "v_pk_fma_f32 v[118:119], v[118:119], v[218:219], v[38:39] op_sel:[0,0,0] op_sel_hi:[1,0,1]\n"
    "v_pk_fma_f32 v[70:71], v[70:71], v[86:87], v[76:77] op_sel:[1,1,0] op_sel_hi:[0,1,1] neg_lo:[0,1,0]\n"
    "v_pk_fma_f32 v[76:77], v[70:71], v[86:87], v[118:119] op_sel:[0,0,0] op_sel_hi:[1,0,1]\n"
    "v_pk_fma_f32 v[70:71], v[70:71], v[86:87], v[76:77] op_sel:[1,1,0] op_sel_hi:[0,1,1] neg_lo:[0,1,0]\n"
    "v_mov_b32 v28, v70\n"
    "v_mov_b32 v30, v70\n"
    "v_mov_b32 v29, v71\n"
    "v_mov_b32 v31, v71\n"
    "v_permlane16_swap_b32 v28, v30\n"
    "s_nop 0\n"
    "v_permlane16_swap_b32 v29, v31\n"
    "v_mov_b32 v72, v28\n"
    "buffer_load_dwordx4 v64, s[20:23], s82 offen offset:3072 lds\n"
    "v_mov_b32 v74, v30\n"
    "v_mov_b32 v73, v29\n"
    "v_mov_b32 v75, v31\n"
    "v_permlane32_swap_b32 v28, v72\n"
    "v_permlane32_swap_b32 v30, v74\n"
    "v_permlane32_swap_b32 v29, v73\n"
    "v_permlane32_swap_b32 v31, v75\n"
    "v_pk_fma_f32 v[76:77], v[28:29], v[176:177], v[30:31] op_sel:[0,0,0] op_sel_hi:[1,0,1]\n"
    "v_pk_fma_f32 v[30:31], v[28:29], v[176:177], v[76:77] op_sel:[1,1,0] op_sel_hi:[0,1,1] neg_lo:[0,1,0]\n"
    "v_pk_fma_f32 v[76:77], v[30:31], v[176:177], v[72:73] op_sel:[0,0,0] op_sel_hi:[1,0,1]\n"
    "v_pk_fma_f32 v[72:73], v[30:31], v[176:177], v[76:77] op_sel:[1,1,0] op_sel_hi:[0,1,1] neg_lo:[0,1,0]\n"
    "v_pk_fma_f32 v[76:77], v[72:73], v[176:177], v[74:75] op_sel:[0,0,0] op_sel_hi:[1,0,1]\n"
    "v_pk_fma_f32 v[88:89], v[72:73], v[176:177], v[76:77] op_sel:[1,1,0] op_sel_hi:[0,1,1] neg_lo:[0,1,0]\n"
    "v_mov_b32 v90, s56\n"
    "v_mov_b32 v91, s57\n"
    "v_lshlrev_b32 v92, 3, v9\n"
    "v_add_u32 v92, 53248, v92\n"
    "s_lshl_b32 s70, s66, 13\n"
    "s_mov_b32 exec_lo, 0xffff\n"
    "s_mov_b32 exec_hi, 0\n"
    "buffer_store_dwordx4 v[88:91], v92, s[16:19], s70 offen sc1\n"
    "s_mov_b64 exec, -1\n"
    "v_cndmask_b32 v94, 0, v28, s[58:59]\n"
    "v_cndmask_b32 v76, v30, v72, s[58:59]\n"
    "v_cndmask_b32 v95, 0, v29, s[58:59]\n"
    "v_cndmask_b32 v77, v31, v73, s[58:59]\n"
    "v_cndmask_b32 v94, v94, v76, s[60:61]\n"
    "v_cndmask_b32 v95, v95, v77, s[60:61]\n"
    "v_lshlrev_b32 v5, 3, v2\n"
    "v_sub_u32 v5, s66, v5\n"
    "v_lshl_add_u32 v6, v5, 13, v92\n"
    "v_mov_b32 v7, 0x7ffffff0\n"
    "v_cmp_lt_i32 s[8:9], 0, v5\n"
    "v_subrev_u32 v9, 8192, v6\n"
    "v_cmp_lt_i32 s[10:11], 1, v5\n"
    "v_subrev_u32 v10, 16384, v6\n"
    "v_cmp_lt_i32 s[12:13], 2, v5\n"
    "v_subrev_u32 v11, 24576, v6\n"
    "v_cmp_lt_i32 s[14:15], 3, v5\n"
    "v_subrev_u32 v28, 32768, v6\n"
    "v_cmp_lt_i32 s[34:35], 4, v5\n"
    "v_subrev_u32 v29, 40960, v6\n"
    "v_cmp_lt_i32 s[40:41], 5, v5\n"
    "v_subrev_u32 v30, 49152, v6\n"
    "v_cmp_lt_i32 s[42:43], 6, v5\n"
    "v_subrev_u32 v31, 57344, v6\n"
    "v_cmp_lt_i32 s[44:45], 7, v5\n"
    "v_subrev_u32 v32, 65536, v6\n"
    "v_cndmask_b32 v9, v7, v9, s[8:9]\n"
    "v_cndmask_b32 v10, v7, v10, s[10:11]\n"
    "v_cndmask_b32 v11, v7, v11, s[12:13]\n"
    "v_cndmask_b32 v28, v7, v28, s[14:15]\n"
    "v_cndmask_b32 v29, v7, v29, s[34:35]\n"
    "v_cndmask_b32 v30, v7, v30, s[40:41]\n"
    "v_cndmask_b32 v31, v7, v31, s[42:43]\n"
    "v_cndmask_b32 v32, v7, v32, s[44:45]\n"
    "v_lshl_add_u32 v5, v2, 2, v59\n"
    "v_bfe_u32 v6, v5, 1, 2\n"
    "v_lshl_add_u32 v5, v5, 7, v60\n"
    "v_add_u32 v5, s73, v5\n"
    "v_lshl_add_u32 v33, v6, 5, v5\n"
    "v_xor_b32 v46, 32, v33\n"
    "v_xor_b32 v47, 64, v33\n"
    "v_xor_b32 v93, 96, v33\n"
    "v_add_u32 v104, 73728, v33\n"
    "v_add_u32 v105, 73728, v46\n"
    "v_add_u32 v106, 73728, v47\n"
    "v_add_u32 v107, 73728, v93\n"
    "s_barrier\n"
    "s_waitcnt vmcnt(25)\n"
    "ds_read_b64_tr_b16 a[128:129], v104 offset:49152\n"
    "ds_read_b64_tr_b16 a[130:131], v104 offset:51200\n"
    "ds_read_b64_tr_b16 a[132:133], v105 offset:49152\n"
    "ds_read_b64_tr_b16 a[134:135], v105 offset:51200\n"
    "ds_read_b64_tr_b16 a[136:137], v106 offset:49152\n"
    "ds_read_b64_tr_b16 a[138:139], v106 offset:51200\n"
    "ds_read_b64_tr_b16 a[140:141], v107 offset:49152\n"
    "ds_read_b64_tr_b16 a[142:143], v107 offset:51200\n"
    "ds_read_b64_tr_b16 a[160:161], v104 offset:32768\n"
    "ds_read_b64_tr_b16 a[162:163], v104 offset:34816\n"
    "ds_read_b64_tr_b16 a[164:165], v105 offset:32768\n"
    "ds_read_b64_tr_b16 a[166:167], v105 offset:34816\n"
    "ds_read_b64_tr_b16 a[168:169], v106 offset:32768\n"
    "ds_read_b64_tr_b16 a[170:171], v106 offset:34816\n"
    "ds_read_b64_tr_b16 a[172:173], v107 offset:32768\n"
    "ds_read_b64_tr_b16 a[174:175], v107 offset:34816\n"
    "ds_read_b64_tr_b16 a[144:145], v33 offset:49152\n"
    "ds_read_b64_tr_b16 a[146:147], v33 offset:51200\n"
    "ds_read_b64_tr_b16 a[148:149], v46 offset:49152\n"
    "ds_read_b64_tr_b16 a[150:151], v46 offset:51200\n"
    "ds_read_b64_tr_b16 a[152:153], v47 offset:49152\n"
    "ds_read_b64_tr_b16 a[154:155], v47 offset:51200\n"
    "ds_read_b64_tr_b16 a[156:157], v93 offset:49152\n"
    "ds_read_b64_tr_b16 a[158:159], v93 offset:51200\n"
    "s_waitcnt vmcnt(21)\n"
    "ds_read_b64_tr_b16 a[176:177], v33 offset:0\n"
    "ds_read_b64_tr_b16 a[178:179], v33 offset:2048\n"
    "ds_read_b64_tr_b16 a[180:181], v46 offset:0\n"
    "ds_read_b64_tr_b16 a[182:183], v46 offset:2048\n"
    "ds_read_b64_tr_b16 a[184:185], v47 offset:0\n"
    "ds_read_b64_tr_b16 a[186:187], v47 offset:2048\n"
    "ds_read_b64_tr_b16 a[188:189], v93 offset:0\n"
    "ds_read_b64_tr_b16 a[190:191], v93 offset:2048\n"
    "s_waitcnt vmcnt(5)\n"
    "s_mov_b64 exec, 1\n"
    "buffer_store_dwordx2 v[56:57], off, s[16:19], s78 sc1\n"
    "s_mov_b64 exec, -1\n"
    "s_waitcnt lgkmcnt(15)\n"
    "v_and_b32 v5, 1, v2\n"
    "v_xor_b32 v5, v5, v58\n"
    "v_lshlrev_b32 v5, 4, v5\n"
    "v_lshl_or_b32 v5, v4, 7, v5\n"
    "v_add_u32 v5, s73, v5\n"
    "v_mov_b32 v6, 122880\n"
    "v_mov_b32 v7, 90112\n"
    "v_cndmask_b32 v6, v6, v7, s[60:61]\n"
    "s_mov_b32 s82, 32768\n"
    "s_add_u32 m0, s73, 122880\n"
    "s_nop 0\n"
    "buffer_load_dwordx4 v65, s[24:27], s82 offen offset:0 lds\n"
    "v_add_u32 v108, v6, v5\n"
    "v_xor_b32 v109, 32, v108\n"
    "v_xor_b32 v110, 64, v108\n"
    "v_xor_b32 v111, 96, v108\n"
    "s_mul_i32 s70, s62, 24576\n"
    "s_lshl_b32 s71, s63, 12\n"
    "s_add_u32 s70, s70, s71\n"
    "s_add_u32 s70, s70, 16384\n"
    "buffer_load_dwordx4 v66, s[24:27], s82 offen offset:1024 lds\n"
    "v_and_b32 v5, 9, v4\n"
    "v_bfe_u32 v6, v4, 1, 1\n"
    "v_lshl_or_b32 v5, v6, 2, v5\n"
    "v_bfe_u32 v6, v4, 2, 1\n"
    "v_lshl_or_b32 v5, v6, 1, v5\n"
    "v_xor_b32 v5, v5, v2\n"
    "v_lshlrev_b32 v5, 3, v5\n"
    "v_lshl_or_b32 v5, v4, 7, v5\n"
    "buffer_load_dwordx4 v67, s[24:27], s82 offen offset:2048 lds\n"
    "v_add_u32 v120, s70, v5\n"
    "v_xor_b32 v121, 32, v120\n"
    "v_xor_b32 v122, 64, v120\n"
    "v_xor_b32 v123, 96, v120\n"
    "v_lshl_add_u32 v7, v2, 3, v59\n"
    "v_and_b32 v5, 9, v7\n"
    "v_bfe_u32 v6, v7, 1, 1\n"
    "v_lshl_or_b32 v5, v6, 2, v5\n"
    "buffer_load_dwordx4 v68, s[24:27], s82 offen offset:3072 lds\n"
    "v_bfe_u32 v6, v7, 2, 1\n"
    "v_lshl_or_b32 v5, v6, 1, v5\n"
    "v_lshlrev_b32 v5, 3, v5\n"
    "v_xor_b32 v5, v5, v60\n"
    "v_lshl_or_b32 v5, v7, 7, v5\n"
    "v_add_u32 v124, s70, v5\n"
    "v_xor_b32 v125, 32, v124\n"
    "v_xor_b32 v126, 64, v124\n"
    "s_mov_b32 s82, 0\n"
    "s_add_u32 m0, s73, 106496\n"
    "s_nop 0\n"
    "buffer_load_dwordx4 v65, s[28:31], s82 offen offset:0 lds\n"
    "v_xor_b32 v127, 96, v124\n"
    "v_lshl_add_u32 v7, v2, 3, v59\n"
    "v_add_u32 v7, 4, v7\n"
    "v_and_b32 v5, 9, v7\n"
    "v_bfe_u32 v6, v7, 1, 1\n"
    "v_lshl_or_b32 v5, v6, 2, v5\n"
    "v_bfe_u32 v6, v7, 2, 1\n"
    "v_lshl_or_b32 v5, v6, 1, v5\n"
    "buffer_load_dwordx4 v66, s[28:31], s82 offen offset:1024 lds\n"
    "v_lshlrev_b32 v5, 3, v5\n"
    "v_xor_b32 v5, v5, v60\n"
    "v_lshl_or_b32 v5, v7, 7, v5\n"
    "v_add_u32 v128, s70, v5\n"
    "v_xor_b32 v129, 32, v128\n"
    "v_xor_b32 v130, 64, v128\n"
    "v_xor_b32 v131, 96, v128\n"
    "buffer_load_dwordx4 v67, s[28:31], s82 offen offset:2048 lds\n"
    "buffer_load_dwordx4 v68, s[28:31], s82 offen offset:3072 lds\n"
    "buffer_load_dwordx4 v[34:37], v9, s[16:19], 0 offen sc1\n"
    "buffer_load_dwordx4 v[38:41], v10, s[16:19], 0 offen sc1\n"
    "buffer_load_dwordx4 v[42:45], v11, s[16:19], 0 offen sc1\n"
    "buffer_load_dwordx4 v[72:75], v28, s[16:19], 0 offen sc1\n"
    "buffer_load_dwordx4 v[78:81], v29, s[16:19], 0 offen sc1\n"
    "buffer_load_dwordx4 v[82:85], v30, s[16:19], 0 offen sc1\n"
    "buffer_load_dwordx4 v[96:99], v31, s[16:19], 0 offen sc1\n"
    "buffer_load_dwordx4 v[100:103], v32, s[16:19], 0 offen sc1\n"
    "s_waitcnt vmcnt(18)\n"
    "ds_read_b64_tr_b16 a[224:225], v104 offset:16384\n"
    "ds_read_b64_tr_b16 a[226:227], v104 offset:18432\n"
    "ds_read_b64_tr_b16 a[228:229], v105 offset:16384\n"
    "ds_read_b64_tr_b16 a[230:231], v105 offset:18432\n"
    "ds_read_b64_tr_b16 a[232:233], v106 offset:16384\n"
    "ds_read_b64_tr_b16 a[234:235], v106 offset:18432\n"
    "ds_read_b64_tr_b16 a[236:237], v107 offset:16384\n"
    "ds_read_b64_tr_b16 a[238:239], v107 offset:18432\n"
    "ds_read_b64_tr_b16 a[192:193], v104 offset:0\n"
    "ds_read_b64_tr_b16 a[194:195], v104 offset:2048\n"
    "ds_read_b64_tr_b16 a[196:197], v105 offset:0\n"
    "ds_read_b64_tr_b16 a[198:199], v105 offset:2048\n"
    "ds_read_b64_tr_b16 a[200:201], v106 offset:0\n"
    "ds_read_b64_tr_b16 a[202:203], v106 offset:2048\n"
    "ds_read_b64_tr_b16 a[204:205], v107 offset:0\n"
    "ds_read_b64_tr_b16 a[206:207], v107 offset:2048\n"
    "s_waitcnt lgkmcnt(8)\n"
    "ds_read_b64_tr_b16 a[208:209], v33 offset:24576\n"
    "ds_read_b64_tr_b16 a[210:211], v33 offset:26624\n"
    "ds_read_b64_tr_b16 a[212:213], v46 offset:24576\n"
    "ds_read_b64_tr_b16 a[214:215], v46 offset:26624\n"
    "ds_read_b64_tr_b16 a[216:217], v47 offset:24576\n"
    "ds_read_b64_tr_b16 a[218:219], v47 offset:26624\n"
    "ds_read_b64_tr_b16 a[220:221], v93 offset:24576\n"
    "ds_read_b64_tr_b16 a[222:223], v93 offset:26624\n"
    "s_mov_b32 s82, 32768\n"
    "s_add_u32 m0, s73, 90112\n"
    "s_nop 0\n"
    "buffer_load_dwordx4 v65, s[28:31], s82 offen offset:0 lds\n"
    "s_xor_b32 s70, s67, 1\n"
    "s_lshl3_add_u32 s52, s67, s69\n"
    "s_lshl_b32 s54, s52, 14\n"
    "s_add_u32 s54, s54, 7786496\n"
    "s_mul_i32 s52, s52, 28672\n"
    "s_add_u32 s52, s52, 446464\n"
    "s_lshl3_add_u32 s47, s70, s69\n"
    "s_lshl_b32 s53, s47, 14\n"
    "buffer_load_dwordx4 v66, s[28:31], s82 offen offset:1024 lds\n"
    "s_add_u32 s53, s53, 7786496\n"
    "s_mul_i32 s47, s47, 28672\n"
    "s_add_u32 s47, s47, 446464\n"
    "s_lshl_b32 s70, s67, 2\n"
    "s_add_u32 s70, s70, s33\n"
    "s_lshl_b32 s70, s70, 4\n"
    "v_add_u32 v133, s70, v2\n"
    "s_lshl_b32 s71, s68, 7\n"
    "buffer_load_dwordx4 v67, s[28:31], s82 offen offset:2048 lds\n"
    "v_lshl_add_u32 v134, v4, 3, s71\n"
    "v_lshl_add_u32 v132, v133, 10, v134\n"
    "v_lshlrev_b32 v135, 1, v132\n"
    "v_lshlrev_b32 v133, 4, v133\n"
    "buffer_load_dwordx4 v68, s[28:31], s82 offen offset:3072 lds\n"
    "buffer_load_dwordx2 v[136:137], v69, s[16:19], s88 offen sc1\n"
    "s_waitcnt vmcnt(5)\n"
    "v_cmp_ne_u64 s[76:77], v[36:37], s[56:57]\n"
    "v_cmp_ne_u64 s[78:79], v[40:41], s[56:57]\n"
    "v_cmp_ne_u64 s[84:85], v[44:45], s[56:57]\n"
    "v_cmp_ne_u64 s[86:87], v[74:75], s[56:57]\n"
    "s_and_b64 s[76:77], s[76:77], s[8:9]\n"
    "s_and_b64 s[78:79], s[78:79], s[10:11]\n"
    "s_and_b64 s[84:85], s[84:85], s[12:13]\n"
    "s_and_b64 s[86:87], s[86:87], s[14:15]\n"
    "s_or_b64 s[76:77], s[76:77], s[78:79]\n"
    "s_or_b64 s[84:85], s[84:85], s[86:87]\n"
    "s_or_b64 s[74:75], s[76:77], s[84:85]\n"
    "v_cmp_ne_u64 s[76:77], v[80:81], s[56:57]\n"
    "v_cmp_ne_u64 s[78:79], v[84:85], s[56:57]\n"
    "v_cmp_ne_u64 s[84:85], v[98:99], s[56:57]\n"
    "v_cmp_ne_u64 s[86:87], v[102:103], s[56:57]\n"
    "s_and_b64 s[76:77], s[76:77], s[34:35]\n"
    "s_and_b64 s[78:79], s[78:79], s[40:41]\n"
    "s_and_b64 s[84:85], s[84:85], s[42:43]\n"
    "s_and_b64 s[86:87], s[86:87], s[44:45]\n"
    "s_or_b64 s[76:77], s[76:77], s[78:79]\n"
    "s_or_b64 s[84:85], s[84:85], s[86:87]\n"
    "s_or_b64 s[76:77], s[76:77], s[84:85]\n"
    "s_or_b64 vcc, s[76:77], s[74:75]\n"
    "s_cbranch_vccnz .Lcar3_%=\n"
    ".Lcab4_%=:\n"
    "v_pk_fma_f32 v[76:77], v[100:101], v[178:179], v[96:97] op_sel:[0,0,0] op_sel_hi:[1,0,1]\n"
    "v_pk_fma_f32 v[96:97], v[100:101], v[178:179], v[76:77] op_sel:[1,1,0] op_sel_hi:[0,1,1] neg_lo:[0,1,0]\n"
    "v_pk_fma_f32 v[76:77], v[96:97], v[178:179], v[82:83] op_sel:[0,0,0] op_sel_hi:[1,0,1]\n"
    "v_pk_fma_f32 v[82:83], v[96:97], v[178:179], v[76:77] op_sel:[1,1,0] op_sel_hi:[0,1,1] neg_lo:[0,1,0]\n"
    "v_pk_fma_f32 v[76:77], v[82:83], v[178:179], v[78:79] op_sel:[0,0,0] op_sel_hi:[1,0,1]\n"
    "v_pk_fma_f32 v[78:79], v[82:83], v[178:179], v[76:77] op_sel:[1,1,0] op_sel_hi:[0,1,1] neg_lo:[0,1,0]\n"
    "v_pk_fma_f32 v[76:77], v[78:79], v[178:179], v[72:73] op_sel:[0,0,0] op_sel_hi:[1,0,1]\n"
    "v_pk_fma_f32 v[72:73], v[78:79], v[178:179], v[76:77] op_sel:[1,1,0] op_sel_hi:[0,1,1] neg_lo:[0,1,0]\n"
    "v_pk_fma_f32 v[76:77], v[72:73], v[178:179], v[42:43] op_sel:[0,0,0] op_sel_hi:[1,0,1]\n"
    "v_pk_fma_f32 v[42:43], v[72:73], v[178:179], v[76:77] op_sel:[1,1,0] op_sel_hi:[0,1,1] neg_lo:[0,1,0]\n"
    "v_pk_fma_f32 v[76:77], v[42:43], v[178:179], v[38:39] op_sel:[0,0,0] op_sel_hi:[1,0,1]\n"
    "v_pk_fma_f32 v[38:39], v[42:43], v[178:179], v[76:77] op_sel:[1,1,0] op_sel_hi:[0,1,1] neg_lo:[0,1,0]\n"
    "v_pk_fma_f32 v[76:77], v[38:39], v[178:179], v[34:35] op_sel:[0,0,0] op_sel_hi:[1,0,1]\n"
    "v_pk_fma_f32 v[34:35], v[38:39], v[178:179], v[76:77] op_sel:[1,1,0] op_sel_hi:[0,1,1] neg_lo:[0,1,0]\n"
    "v_pk_mul_f32 v[76:77], v[34:35], v[180:181] op_sel:[0,0] op_sel_hi:[1,0]\n"
    "v_pk_fma_f32 v[70:71], v[34:35], v[180:181], v[76:77] op_sel:[1,1,0] op_sel_hi:[0,1,1] neg_lo:[0,1,0]\n"
    "v_mov_b32 v76, v70\n"
    "v_mov_b32 v77, v71\n"
    "s_nop 0\n"
    "v_permlane16_swap_b32 v70, v76\n"
    "v_permlane16_swap_b32 v71, v77\n"
    "v_pk_add_f32 v[70:71], v[70:71], v[76:77]\n"
    "v_mov_b32 v76, v70\n"
    "v_mov_b32 v77, v71\n"
    "s_nop 0\n"
    "v_permlane32_swap_b32 v70, v76\n"
    "v_permlane32_swap_b32 v71, v77\n"
    "v_pk_add_f32 v[70:71], v[70:71], v[76:77]\n"
    "v_pk_fma_f32 v[76:77], v[70:71], v[182:183], v[94:95] op_sel:[0,0,0] op_sel_hi:[1,0,1]\n"
    "v_pk_fma_f32 v[70:71], v[70:71], v[182:183], v[76:77] op_sel:[1,1,0] op_sel_hi:[0,1,1] neg_lo:[0,1,0]\n"
    "s_waitcnt vmcnt(0)\n"
    "v_cmp_ne_u64 vcc, v[136:137], s[56:57]\n"
    "s_cbranch_vccnz .Lfmr5_%=\n"
    ".Lfmb6_%=:\n"
    "v_pk_fma_f32 v[76:77], v[70:71], v[86:87], v[12:13] op_sel:[0,0,0] op_sel_hi:[1,0,1]\n"
    "v_pk_fma_f32 v[12:13], v[70:71], v[86:87], v[76:77] op_sel:[1,1,0] op_sel_hi:[0,1,1] neg_lo:[0,1,0]\n"
    "v_pk_fma_f32 v[76:77], v[12:13], v[86:87], v[14:15] op_sel:[0,0,0] op_sel_hi:[1,0,1]\n"
    "v_pk_fma_f32 v[14:15], v[12:13], v[86:87], v[76:77] op_sel:[1,1,0] op_sel_hi:[0,1,1] neg_lo:[0,1,0]\n"
    "v_pk_fma_f32 v[76:77], v[14:15], v[86:87], v[16:17] op_sel:[0,0,0] op_sel_hi:[1,0,1]\n"
    "s_add_u32 s46, s47, 0\n"
    "buffer_load_dwordx4 v[88:91], v3, s[16:19], s46 offen offset:0 sc1\n"
    "v_pk_fma_f32 v[16:17], v[14:15], v[86:87], v[76:77] op_sel:[1,1,0] op_sel_hi:[0,1,1] neg_lo:[0,1,0]\n"
    "v_cvt_pk_bf16_f32 v34, v12, v14\n"
    "v_cvt_pk_bf16_f32 v42, -v13, -v15\n"
    "v_pk_fma_f32 v[76:77], v[16:17], v[86:87], v[18:19] op_sel:[0,0,0] op_sel_hi:[1,0,1]\n"
    "v_pk_fma_f32 v[18:19], v[16:17], v[86:87], v[76:77] op_sel:[1,1,0] op_sel_hi:[0,1,1] neg_lo:[0,1,0]\n"
    "buffer_load_dwordx4 v[104:107], v3, s[16:19], s46 offen offset:1024 sc1\n"
    "v_pk_fma_f32 v[76:77], v[18:19], v[86:87], v[20:21] op_sel:[0,0,0] op_sel_hi:[1,0,1]\n"
    "v_pk_fma_f32 v[20:21], v[18:19], v[86:87], v[76:77] op_sel:[1,1,0] op_sel_hi:[0,1,1] neg_lo:[0,1,0]\n"
    "v_cvt_pk_bf16_f32 v35, v16, v18\n"
    "v_cvt_pk_bf16_f32 v43, -v17, -v19\n"
    "ds_write_b64 v120, v[34:35] offset:0\n"
    "buffer_load_dwordx4 v[138:141], v3, s[16:19], s46 offen offset:2048 sc1\n"
    "ds_write_b64 v120, v[42:43] offset:2048\n"
    "v_pk_fma_f32 v[76:77], v[20:21], v[86:87], v[22:23] op_sel:[0,0,0] op_sel_hi:[1,0,1]\n"
    "v_pk_fma_f32 v[22:23], v[20:21], v[86:87], v[76:77] op_sel:[1,1,0] op_sel_hi:[0,1,1] neg_lo:[0,1,0]\n"
    "v_pk_fma_f32 v[76:77], v[22:23], v[86:87], v[24:25] op_sel:[0,0,0] op_sel_hi:[1,0,1]\n"
    "v_pk_fma_f32 v[24:25], v[22:23], v[86:87], v[76:77] op_sel:[1,1,0] op_sel_hi:[0,1,1] neg_lo:[0,1,0]\n"
    "buffer_load_dwordx4 v[142:145], v3, s[16:19], s46 offen offset:3072 sc1\n"
    "v_cvt_pk_bf16_f32 v36, v20, v22\n"
    "v_cvt_pk_bf16_f32 v44, -v21, -v23\n"
    "v_pk_fma_f32 v[76:77], v[24:25], v[86:87], v[26:27] op_sel:[0,0,0] op_sel_hi:[1,0,1]\n"
    "v_pk_fma_f32 v[26:27], v[24:25], v[86:87], v[76:77] op_sel:[1,1,0] op_sel_hi:[0,1,1] neg_lo:[0,1,0]\n"
    "v_pk_fma_f32 v[76:77], v[26:27], v[86:87], v[48:49] op_sel:[0,0,0] op_sel_hi:[1,0,1]\n"
    "s_add_u32 s46, s47, 4096\n"
    "buffer_load_dwordx4 v[146:149], v3, s[16:19], s46 offen offset:0 sc1\n"
    "v_pk_fma_f32 v[48:49], v[26:27], v[86:87], v[76:77] op_sel:[1,1,0] op_sel_hi:[0,1,1] neg_lo:[0,1,0]\n"
    "v_cvt_pk_bf16_f32 v37, v24, v26\n"
    "v_cvt_pk_bf16_f32 v45, -v25, -v27\n"
    "ds_write_b64 v121, v[36:37] offset:0\n"
    "ds_write_b64 v121, v[44:45] offset:2048\n"
    "buffer_load_dwordx4 v[150:153], v3, s[16:19], s46 offen offset:1024 sc1\n"
    "v_pk_fma_f32 v[76:77], v[48:49], v[86:87], v[50:51] op_sel:[0,0,0] op_sel_hi:[1,0,1]\n"
    "v_pk_fma_f32 v[50:51], v[48:49], v[86:87], v[76:77] op_sel:[1,1,0] op_sel_hi:[0,1,1] neg_lo:[0,1,0]\n"
    "v_pk_fma_f32 v[76:77], v[50:51], v[86:87], v[52:53] op_sel:[0,0,0] op_sel_hi:[1,0,1]\n"
    "v_pk_fma_f32 v[52:53], v[50:51], v[86:87], v[76:77] op_sel:[1,1,0] op_sel_hi:[0,1,1] neg_lo:[0,1,0]\n"
    "v_cvt_pk_bf16_f32 v38, v48, v50\n"
    "buffer_load_dwordx4 v[154:157], v3, s[16:19], s46 offen offset:2048 sc1\n"
    "v_cvt_pk_bf16_f32 v72, -v49, -v51\n"
    "v_pk_fma_f32 v[76:77], v[52:53], v[86:87], v[54:55] op_sel:[0,0,0] op_sel_hi:[1,0,1]\n"
    "v_pk_fma_f32 v[54:55], v[52:53], v[86:87], v[76:77] op_sel:[1,1,0] op_sel_hi:[0,1,1] neg_lo:[0,1,0]\n"
    "v_pk_fma_f32 v[76:77], v[54:55], v[86:87], v[112:113] op_sel:[0,0,0] op_sel_hi:[1,0,1]\n"
    "v_pk_fma_f32 v[112:113], v[54:55], v[86:87], v[76:77] op_sel:[1,1,0] op_sel_hi:[0,1,1] neg_lo:[0,1,0]\n"
    "buffer_load_dwordx4 v[158:161], v3, s[16:19], s46 offen offset:3072 sc1\n"
    "v_cvt_pk_bf16_f32 v39, v52, v54\n"
    "v_cvt_pk_bf16_f32 v73, -v53, -v55\n"
    "ds_write_b64 v122, v[38:39] offset:0\n"
    "ds_write_b64 v122, v[72:73] offset:2048\n"
    "v_pk_fma_f32 v[76:77], v[112:113], v[86:87], v[114:115] op_sel:[0,0,0] op_sel_hi:[1,0,1]\n"
    "v_pk_fma_f32 v[114:115], v[112:113], v[86:87], v[76:77] op_sel:[1,1,0] op_sel_hi:[0,1,1] neg_lo:[0,1,0]\n"
    "v_pk_fma_f32 v[76:77], v[114:115], v[86:87], v[116:117] op_sel:[0,0,0] op_sel_hi:[1,0,1]\n"
    "v_pk_fma_f32 v[116:117], v[114:115], v[86:87], v[76:77] op_sel:[1,1,0] op_sel_hi:[0,1,1] neg_lo:[0,1,0]\n"
    "v_cvt_pk_bf16_f32 v40, v112, v114\n"
    "v_cvt_pk_bf16_f32 v74, -v113, -v115\n"
    "v_pk_fma_f32 v[76:77], v[116:117], v[86:87], v[118:119] op_sel:[0,0,0] op_sel_hi:[1,0,1]\n"
    "v_pk_fma_f32 v[118:119], v[116:117], v[86:87], v[76:77] op_sel:[1,1,0] op_sel_hi:[0,1,1] neg_lo:[0,1,0]\n"
    "v_cvt_pk_bf16_f32 v41, v116, v118\n"
    "v_cvt_pk_bf16_f32 v75, -v117, -v119\n"
    "ds_write_b64 v123, v[40:41] offset:0\n"
    "ds_write_b64 v123, v[74:75] offset:2048\n"
    "ds_read_b64_tr_b16 v[78:79], v124\n"
    "ds_read_b64_tr_b16 v[80:81], v128\n"
    "ds_read_b64_tr_b16 v[82:83], v125\n"
    "ds_read_b64_tr_b16 v[84:85], v129\n"
    "ds_read_b64_tr_b16 v[96:97], v126\n"
    "ds_read_b64_tr_b16 v[98:99], v130\n"
    "ds_read_b64_tr_b16 v[100:101], v127\n"
    "ds_read_b64_tr_b16 v[102:103], v131\n"
    "s_waitcnt lgkmcnt(0)\n"
    "buffer_store_dwordx4 v[78:81], v3, s[16:19], s64 offen offset:0 sc1\n"
    "buffer_store_dwordx4 v[82:85], v3, s[16:19], s64 offen offset:1024 sc1\n"
    "buffer_store_dwordx4 v[96:99], v3, s[16:19], s64 offen offset:2048 sc1\n"
    "buffer_store_dwordx4 v[100:103], v3, s[16:19], s64 offen offset:3072 sc1\n"
    "s_waitcnt vmcnt(8)\n"
    "v_mfma_f32_16x16x32_bf16 a[0:3], a[128:131], v[88:91], 0\n"
    "s_add_u32 s46, s47, 8192\n"
    "v_and_b32 v5, 7, v4\n"
    "v_xor_b32 v5, v5, v2\n"
    "v_mfma_f32_16x16x32_bf16 a[16:19], a[132:135], v[88:91], 0\n"
    "buffer_load_dwordx4 v[162:165], v3, s[16:19], s46 offen offset:0 sc1\n"
    "v_mfma_f32_16x16x32_bf16 a[32:35], a[136:139], v[88:91], 0\n"
    "v_lshlrev_b32 v5, 4, v5\n"
    "v_and_b32 v6, 3, v4\n"
    "v_mfma_f32_16x16x32_bf16 a[48:51], a[140:143], v[88:91], 0\n"
    "buffer_load_dwordx4 v[166:169], v3, s[16:19], s46 offen offset:1024 sc1\n"
    "v_mfma_f32_16x16x32_bf16 a[4:7], a[128:131], v[104:107], 0\n"
    "v_lshl_or_b32 v5, v6, 8, v5\n"
    "v_add_u32 v5, s73, v5\n"
    "v_mfma_f32_16x16x32_bf16 a[20:23], a[132:135], v[104:107], 0\n"
    "buffer_load_dwordx4 v[170:173], v3, s[16:19], s46 offen offset:2048 sc1\n"
    "v_mfma_f32_16x16x32_bf16 a[36:39], a[136:139], v[104:107], 0\n"
    "v_lshrrev_b32 v6, 2, v4\n"
    "v_lshl_add_u32 v47, v6, 14, v5\n"
    "v_mfma_f32_16x16x32_bf16 a[52:55], a[140:143], v[104:107], 0\n"
    "buffer_load_dwordx4 v[174:177], v3, s[16:19], s46 offen offset:3072 sc1\n"
    "v_mfma_f32_16x16x32_bf16 a[8:11], a[128:131], v[138:141], 0\n"
    "s_add_u32 s46, s47, 12288\n"
    "v_lshl_add_u32 v33, v6, 13, v47\n"
    "v_add_u32 v47, 90112, v47\n"
    "v_mfma_f32_16x16x32_bf16 a[24:27], a[132:135], v[138:141], 0\n"
    "buffer_load_dwordx4 v[184:187], v3, s[16:19], s46 offen offset:0 sc1\n"
    "v_mfma_f32_16x16x32_bf16 a[40:43], a[136:139], v[138:141], 0\n"
    "v_xor_b32 v46, 64, v33\n"
    "v_xor_b32 v58, 64, v47\n"
    "v_mfma_f32_16x16x32_bf16 a[56:59], a[140:143], v[138:141], 0\n"
    "buffer_load_dwordx4 v[188:191], v3, s[16:19], s46 offen offset:1024 sc1\n"
    "v_mfma_f32_16x16x32_bf16 a[12:15], a[128:131], v[142:145], 0\n"
    "s_lshl_b32 s70, s63, 2\n"
    "v_or_b32 v5, s70, v2\n"
    "v_xor_b32 v5, v5, v4\n"
    "v_mfma_f32_16x16x32_bf16 a[28:31], a[132:135], v[142:145], 0\n"
    "buffer_load_dwordx4 v[192:195], v3, s[16:19], s46 offen offset:2048 sc1\n"
    "v_mfma_f32_16x16x32_bf16 a[44:47], a[136:139], v[142:145], 0\n"
    "v_lshlrev_b32 v5, 4, v5\n"
    "v_lshl_or_b32 v5, v2, 8, v5\n"
    "v_mfma_f32_16x16x32_bf16 a[60:63], a[140:143], v[142:145], 0\n"
    "buffer_load_dwordx4 v[196:199], v3, s[16:19], s46 offen offset:3072 sc1\n"
    "s_waitcnt vmcnt(12)\n"
    "v_mfma_f32_16x16x32_bf16 a[0:3], a[144:147], v[146:149], a[0:3]\n"
    "s_add_u32 s46, s47, 16384\n"
    "s_mul_i32 s70, s33, 24576\n"
    "v_add_u32 v59, s70, v5\n"
    "v_mfma_f32_16x16x32_bf16 a[16:19], a[148:151], v[146:149], a[16:19]\n"
    "buffer_load_dwordx4 v[200:203], v3, s[16:19], s46 offen offset:0 sc1\n"
    "v_mfma_f32_16x16x32_bf16 a[32:35], a[152:155], v[146:149], a[32:35]\n"
    "s_lshl_b32 s22, s33, 13\n"
    "s_sub_u32 s22, 90112, s22\n"
    "s_lshl_b32 s20, %1, 2\n"
    "v_mfma_f32_16x16x32_bf16 a[48:51], a[156:159], v[146:149], a[48:51]\n"
    "buffer_load_dwordx4 v[204:207], v3, s[16:19], s46 offen offset:1024 sc1\n"
    "v_mfma_f32_16x16x32_bf16 a[4:7], a[144:147], v[150:153], a[4:7]\n"
    "s_add_u32 s20, s20, s33\n"
    "s_lshl_b32 s20, s20, 12\n"
    "s_xor_b32 s21, s20, 16384\n"
    "v_mfma_f32_16x16x32_bf16 a[20:23], a[148:151], v[150:153], a[20:23]\n"
    "buffer_load_dwordx4 v[208:211], v3, s[16:19], s46 offen offset:2048 sc1\n"
    "v_mfma_f32_16x16x32_bf16 a[36:39], a[152:155], v[150:153], a[36:39]\n"
    "s_add_u32 s20, s20, 11980800\n"
    "s_add_u32 s21, s21, 11980800\n"
    "v_mfma_f32_16x16x32_bf16 a[52:55], a[156:159], v[150:153], a[52:55]\n"
    "buffer_load_dwordx4 v[212:215], v3, s[16:19], s46 offen offset:3072 sc1\n"
    "v_mfma_f32_16x16x32_bf16 a[8:11], a[144:147], v[154:157], a[8:11]\n"
    "s_add_u32 s46, s47, 20480\n"
    "v_mfma_f32_16x16x32_bf16 a[24:27], a[148:151], v[154:157], a[24:27]\n"
    "buffer_load_dwordx4 v[88:91], v3, s[16:19], s46 offen offset:0 sc1\n"
    "v_mfma_f32_16x16x32_bf16 a[40:43], a[152:155], v[154:157], a[40:43]\n"
    "v_mfma_f32_16x16x32_bf16 a[56:59], a[156:159], v[154:157], a[56:59]\n"
    "buffer_load_dwordx4 v[104:107], v3, s[16:19], s46 offen offset:1024 sc1\n"
    "v_mfma_f32_16x16x32_bf16 a[12:15], a[144:147], v[158:161], a[12:15]\n"
    "v_mfma_f32_16x16x32_bf16 a[28:31], a[148:151], v[158:161], a[28:31]\n"
    "buffer_load_dwordx4 v[138:141], v3, s[16:19], s46 offen offset:2048 sc1\n"
    "v_mfma_f32_16x16x32_bf16 a[44:47], a[152:155], v[158:161], a[44:47]\n"
    "v_mfma_f32_16x16x32_bf16 a[60:63], a[156:159], v[158:161], a[60:63]\n"
    "buffer_load_dwordx4 v[142:145], v3, s[16:19], s46 offen offset:3072 sc1\n"
    "s_waitcnt vmcnt(12)\n"
    "s_waitcnt vmcnt(16)\n"
    "s_mov_b64 exec, 1\n"
    "buffer_store_dwordx2 v[56:57], off, s[16:19], s81 sc1\n"
    "s_mov_b64 exec, -1\n"
    "v_mfma_f32_16x16x32_bf16 a[0:3], a[160:163], v[162:165], a[0:3]\n"
    "s_add_u32 s46, s47, 24576\n"
    "s_mov_b32 s8, 4096\n"
    "v_mfma_f32_16x16x32_bf16 a[16:19], a[164:167], v[162:165], a[16:19]\n"
    "buffer_load_dwordx4 v[146:149], v3, s[16:19], s46 offen offset:0 sc1\n"
    "v_mfma_f32_16x16x32_bf16 a[32:35], a[168:171], v[162:165], a[32:35]\n"
    "buffer_load_dwordx2 v[120:121], v133, s[16:19], s8 offen offset:0 sc1\n"
    "v_mfma_f32_16x16x32_bf16 a[48:51], a[172:175], v[162:165], a[48:51]\n"
    "buffer_load_dwordx4 v[150:153], v3, s[16:19], s46 offen offset:1024 sc1\n"
    "v_mfma_f32_16x16x32_bf16 a[4:7], a[160:163], v[166:169], a[4:7]\n"
    "buffer_load_dwordx2 v[122:123], v133, s[16:19], s8 offen offset:64 sc1\n"
    "v_mfma_f32_16x16x32_bf16 a[20:23], a[164:167], v[166:169], a[20:23]\n"
    "buffer_load_dwordx4 v[154:157], v3, s[16:19], s46 offen offset:2048 sc1\n"
    "v_mfma_f32_16x16x32_bf16 a[36:39], a[168:171], v[166:169], a[36:39]\n"
    "buffer_load_dwordx2 v[124:125], v133, s[16:19], s8 offen offset:128 sc1\n"
    "v_mfma_f32_16x16x32_bf16 a[52:55], a[172:175], v[166:169], a[52:55]\n"
    "buffer_load_dwordx4 v[158:161], v3, s[16:19], s46 offen offset:3072 sc1\n"
    "v_mfma_f32_16x16x32_bf16 a[8:11], a[160:163], v[170:173], a[8:11]\n"
    "buffer_load_dwordx2 v[126:127], v133, s[16:19], s8 offen offset:192 sc1\n"
    "v_mfma_f32_16x16x32_bf16 a[24:27], a[164:167], v[170:173], a[24:27]\n"
    "global_load_dwordx2 v[128:129], v134, s[48:49]\n"
    "v_mfma_f32_16x16x32_bf16 a[40:43], a[168:171], v[170:173], a[40:43]\n"
    "global_load_dwordx2 v[130:131], v134, s[50:51]\n"
    "v_mfma_f32_16x16x32_bf16 a[56:59], a[172:175], v[170:173], a[56:59]\n"
    "s_add_u32 s46, s52, 0\n"
    "s_movk_i32 s8, 0\n"
    "v_mfma_f32_16x16x32_bf16 a[12:15], a[160:163], v[174:177], a[12:15]\n"
    "buffer_load_dwordx4 v[162:165], v3, s[16:19], s46 offen offset:0 sc1\n"
    "v_mfma_f32_16x16x32_bf16 a[28:31], a[164:167], v[174:177], a[28:31]\n"
    "buffer_load_dwordx4 v[166:169], v3, s[16:19], s46 offen offset:1024 sc1\n"
    "v_mfma_f32_16x16x32_bf16 a[44:47], a[168:171], v[174:177], a[44:47]\n"
    "buffer_load_dwordx4 v[170:173], v3, s[16:19], s46 offen offset:2048 sc1\n"
    "v_mfma_f32_16x16x32_bf16 a[60:63], a[172:175], v[174:177], a[60:63]\n"
    "buffer_load_dwordx4 v[174:177], v3, s[16:19], s46 offen offset:3072 sc1\n"
    "s_waitcnt vmcnt(23)\n"
    "v_mfma_f32_16x16x32_bf16 a[0:3], a[176:179], v[184:187], a[0:3]\n"
    "buffer_load_dwordx2 v[112:113], v132, s[4:7], s8 offen\n"
    "v_mfma_f32_16x16x32_bf16 a[16:19], a[180:183], v[184:187], a[16:19]\n"
    "s_add_u32 s46, s52, 4096\n"
    "s_movk_i32 s8, 4096\n"
    "v_mfma_f32_16x16x32_bf16 a[32:35], a[184:187], v[184:187], a[32:35]\n"
    "buffer_load_dwordx2 v[114:115], v132, s[4:7], s8 offen\n"
    "v_mfma_f32_16x16x32_bf16 a[48:51], a[188:191], v[184:187], a[48:51]\n"
    "buffer_load_dwordx4 v[184:187], v3, s[16:19], s46 offen offset:0 sc1\n"
    "v_mfma_f32_16x16x32_bf16 a[4:7], a[176:179], v[188:191], a[4:7]\n"
    "s_movk_i32 s8, 8192\n"
    "v_mfma_f32_16x16x32_bf16 a[20:23], a[180:183], v[188:191], a[20:23]\n"
    "buffer_load_dwordx2 v[116:117], v132, s[4:7], s8 offen\n"
    "v_mfma_f32_16x16x32_bf16 a[36:39], a[184:187], v[188:191], a[36:39]\n"
    "s_movk_i32 s8, 12288\n"
    "v_mfma_f32_16x16x32_bf16 a[52:55], a[188:191], v[188:191], a[52:55]\n"
    "buffer_load_dwordx4 v[188:191], v3, s[16:19], s46 offen offset:1024 sc1\n"
    "v_mfma_f32_16x16x32_bf16 a[8:11], a[176:179], v[192:195], a[8:11]\n"
    "buffer_load_dwordx2 v[118:119], v132, s[4:7], s8 offen\n"
    "v_mfma_f32_16x16x32_bf16 a[24:27], a[180:183], v[192:195], a[24:27]\n"
    "v_mfma_f32_16x16x32_bf16 a[40:43], a[184:187], v[192:195], a[40:43]\n"
    "v_mfma_f32_16x16x32_bf16 a[56:59], a[188:191], v[192:195], a[56:59]\n"
    "buffer_load_dwordx4 v[192:195], v3, s[16:19], s46 offen offset:2048 sc1\n"
    "v_mfma_f32_16x16x32_bf16 a[12:15], a[176:179], v[196:199], a[12:15]\n"
    "v_mfma_f32_16x16x32_bf16 a[28:31], a[180:183], v[196:199], a[28:31]\n"
    "v_mfma_f32_16x16x32_bf16 a[44:47], a[184:187], v[196:199], a[44:47]\n"
    "v_mfma_f32_16x16x32_bf16 a[60:63], a[188:191], v[196:199], a[60:63]\n"
    "buffer_load_dwordx4 v[196:199], v3, s[16:19], s46 offen offset:3072 sc1\n"
    "s_waitcnt vmcnt(27)\n"
    "v_mfma_f32_16x16x32_bf16 a[0:3], a[192:195], v[200:203], a[0:3]\n"
    "v_mfma_f32_16x16x32_bf16 a[16:19], a[196:199], v[200:203], a[16:19]\n"
    "s_add_u32 s46, s52, 8192\n"
    "v_mfma_f32_16x16x32_bf16 a[32:35], a[200:203], v[200:203], a[32:35]\n"
    "v_mfma_f32_16x16x32_bf16 a[48:51], a[204:207], v[200:203], a[48:51]\n"
    "buffer_load_dwordx4 v[200:203], v3, s[16:19], s46 offen offset:0 sc1\n"
    "v_mfma_f32_16x16x32_bf16 a[4:7], a[192:195], v[204:207], a[4:7]\n"
    "v_mfma_f32_16x16x32_bf16 a[20:23], a[196:199], v[204:207], a[20:23]\n"
    "v_mfma_f32_16x16x32_bf16 a[36:39], a[200:203], v[204:207], a[36:39]\n"
    "v_mfma_f32_16x16x32_bf16 a[52:55], a[204:207], v[204:207], a[52:55]\n"
    "buffer_load_dwordx4 v[204:207], v3, s[16:19], s46 offen offset:1024 sc1\n"
    "v_mfma_f32_16x16x32_bf16 a[8:11], a[192:195], v[208:211], a[8:11]\n"
    "v_mfma_f32_16x16x32_bf16 a[24:27], a[196:199], v[208:211], a[24:27]\n"
    "v_mfma_f32_16x16x32_bf16 a[40:43], a[200:203], v[208:211], a[40:43]\n"
    "v_mfma_f32_16x16x32_bf16 a[56:59], a[204:207], v[208:211], a[56:59]\n"
    "buffer_load_dwordx4 v[208:211], v3, s[16:19], s46 offen offset:2048 sc1\n"
    "v_mfma_f32_16x16x32_bf16 a[12:15], a[192:195], v[212:215], a[12:15]\n"
    "v_mfma_f32_16x16x32_bf16 a[28:31], a[196:199], v[212:215], a[28:31]\n"
    "v_mfma_f32_16x16x32_bf16 a[44:47], a[200:203], v[212:215], a[44:47]\n"
    "v_mfma_f32_16x16x32_bf16 a[60:63], a[204:207], v[212:215], a[60:63]\n"
    "buffer_load_dwordx4 v[212:215], v3, s[16:19], s46 offen offset:3072 sc1\n"
    "s_waitcnt vmcnt(27)\n"
    "v_mfma_f32_16x16x32_bf16 a[0:3], a[208:211], v[88:91], a[0:3]\n"
    "s_waitcnt vmcnt(6)\n"
    "v_lshlrev_b32 v100, 16, v128\n"
    "v_and_b32 v101, 0xffff0000, v128\n"
    "v_mfma_f32_16x16x32_bf16 a[16:19], a[212:215], v[88:91], a[16:19]\n"
    "s_add_u32 s46, s52, 12288\n"
    "v_lshlrev_b32 v102, 16, v129\n"
    "v_and_b32 v103, 0xffff0000, v129\n"
    "v_mfma_f32_16x16x32_bf16 a[32:35], a[216:219], v[88:91], a[32:35]\n"
    "v_lshlrev_b32 v178, 16, v130\n"
    "v_and_b32 v179, 0xffff0000, v130\n"
    "v_mfma_f32_16x16x32_bf16 a[48:51], a[220:223], v[88:91], a[48:51]\n"
    "buffer_load_dwordx4 v[88:91], v3, s[16:19], s46 offen offset:0 sc1\n"
    "v_mfma_f32_16x16x32_bf16 a[4:7], a[208:211], v[104:107], a[4:7]\n"
    "v_lshlrev_b32 v180, 16, v131\n"
    "v_and_b32 v181, 0xffff0000, v131\n"
    "v_mfma_f32_16x16x32_bf16 a[20:23], a[212:215], v[104:107], a[20:23]\n"
    "v_lshlrev_b32 v216, 16, v112\n"
    "v_and_b32 v217, 0xffff0000, v112\n"
    "v_mfma_f32_16x16x32_bf16 a[36:39], a[216:219], v[104:107], a[36:39]\n"
    "v_lshlrev_b32 v218, 16, v113\n"
    "v_and_b32 v219, 0xffff0000, v113\n"
    "v_mfma_f32_16x16x32_bf16 a[52:55], a[220:223], v[104:107], a[52:55]\n"
    "buffer_load_dwordx4 v[104:107], v3, s[16:19], s46 offen offset:1024 sc1\n"
    "v_mfma_f32_16x16x32_bf16 a[8:11], a[208:211], v[138:141], a[8:11]\n"
    "v_add_f32 v78, v216, v178\n"
    "v_add_f32 v79, v217, v179\n"
    "v_mfma_f32_16x16x32_bf16 a[24:27], a[212:215], v[138:141], a[24:27]\n"
    "v_add_f32 v80, v218, v180\n"
    "v_add_f32 v81, v219, v181\n"
    "v_mfma_f32_16x16x32_bf16 a[40:43], a[216:219], v[138:141], a[40:43]\n"
    "v_fma_f32 v216, v216, v120, -v121\n"
    "v_fma_f32 v217, v217, v120, -v121\n"
    "v_mfma_f32_16x16x32_bf16 a[56:59], a[220:223], v[138:141], a[56:59]\n"
    "buffer_load_dwordx4 v[138:141], v3, s[16:19], s46 offen offset:2048 sc1\n"
    "v_mfma_f32_16x16x32_bf16 a[12:15], a[208:211], v[142:145], a[12:15]\n"
    "v_fma_f32 v218, v218, v120, -v121\n"
    "v_fma_f32 v219, v219, v120, -v121\n"
    "v_mfma_f32_16x16x32_bf16 a[28:31], a[212:215], v[142:145], a[28:31]\n"
    "v_fmac_f32 v78, v216, v100\n"
    "v_fmac_f32 v79, v217, v101\n"
    "v_mfma_f32_16x16x32_bf16 a[44:47], a[216:219], v[142:145], a[44:47]\n"
    "v_fmac_f32 v80, v218, v102\n"
    "v_fmac_f32 v81, v219, v103\n"
    "v_mfma_f32_16x16x32_bf16 a[60:63], a[220:223], v[142:145], a[60:63]\n"
    "buffer_load_dwordx4 v[142:145], v3, s[16:19], s46 offen offset:3072 sc1\n"
    "s_waitcnt vmcnt(23)\n"
    "v_mfma_f32_16x16x32_bf16 a[0:3], a[224:227], v[146:149], a[0:3]\n"
    "buffer_load_dwordx2 v[136:137], v69, s[16:19], s89 offen sc1\n"
    "v_mfma_f32_16x16x32_bf16 a[16:19], a[228:231], v[146:149], a[16:19]\n"
    "v_lshlrev_b32 v216, 16, v114\n"
    "v_and_b32 v217, 0xffff0000, v114\n"
    "v_mfma_f32_16x16x32_bf16 a[32:35], a[232:235], v[146:149], a[32:35]\n"
    "v_lshlrev_b32 v218, 16, v115\n"
    "v_and_b32 v219, 0xffff0000, v115\n"
    "v_mfma_f32_16x16x32_bf16 a[48:51], a[236:239], v[146:149], a[48:51]\n"
    "v_add_f32 v82, v216, v178\n"
    "v_add_f32 v83, v217, v179\n"
    "v_mfma_f32_16x16x32_bf16 a[4:7], a[224:227], v[150:153], a[4:7]\n"
    "v_add_f32 v84, v218, v180\n"
    "v_add_f32 v85, v219, v181\n"
    "v_mfma_f32_16x16x32_bf16 a[20:23], a[228:231], v[150:153], a[20:23]\n"
    "v_fma_f32 v216, v216, v122, -v123\n"
    "v_fma_f32 v217, v217, v122, -v123\n"
    "v_mfma_f32_16x16x32_bf16 a[36:39], a[232:235], v[150:153], a[36:39]\n"
    "v_fma_f32 v218, v218, v122, -v123\n"
    "v_fma_f32 v219, v219, v122, -v123\n"
    "v_mfma_f32_16x16x32_bf16 a[52:55], a[236:239], v[150:153], a[52:55]\n"
    "v_fmac_f32 v82, v216, v100\n"
    "v_fmac_f32 v83, v217, v101\n"
    "v_mfma_f32_16x16x32_bf16 a[8:11], a[224:227], v[154:157], a[8:11]\n"
    "v_fmac_f32 v84, v218, v102\n"
    "v_fmac_f32 v85, v219, v103\n"
    "v_mfma_f32_16x16x32_bf16 a[24:27], a[228:231], v[154:157], a[24:27]\n"
    "v_lshlrev_b32 v216, 16, v116\n"
    "v_and_b32 v217, 0xffff0000, v116\n"
    "v_mfma_f32_16x16x32_bf16 a[40:43], a[232:235], v[154:157], a[40:43]\n"
    "v_lshlrev_b32 v218, 16, v117\n"
    "v_and_b32 v219, 0xffff0000, v117\n"
    "v_mfma_f32_16x16x32_bf16 a[56:59], a[236:239], v[154:157], a[56:59]\n"
    "v_add_f32 v92, v216, v178\n"
    "v_add_f32 v93, v217, v179\n"
    "v_mfma_f32_16x16x32_bf16 a[12:15], a[224:227], v[158:161], a[12:15]\n"
    "v_add_f32 v94, v218, v180\n"
    "v_add_f32 v95, v219, v181\n"
    "v_mfma_f32_16x16x32_bf16 a[28:31], a[228:231], v[158:161], a[28:31]\n"
    "v_fma_f32 v216, v216, v124, -v125\n"
    "v_fma_f32 v217, v217, v124, -v125\n"
    "v_mfma_f32_16x16x32_bf16 a[44:47], a[232:235], v[158:161], a[44:47]\n"
    "v_fma_f32 v218, v218, v124, -v125\n"
    "v_fma_f32 v219, v219, v124, -v125\n"
    "v_mfma_f32_16x16x32_bf16 a[60:63], a[236:239], v[158:161], a[60:63]\n"
    "v_fmac_f32 v92, v216, v100\n"
    "v_fmac_f32 v93, v217, v101\n"
    "s_waitcnt vmcnt(17)\n"
    "v_mfma_f32_16x16x32_bf16 a[64:67], a[128:131], v[162:165], 0\n"
    "v_fmac_f32 v94, v218, v102\n"
    "v_fmac_f32 v95, v219, v103\n"
    "v_mfma_f32_16x16x32_bf16 a[80:83], a[132:135], v[162:165], 0\n"
    "v_lshlrev_b32 v216, 16, v118\n"
    "v_and_b32 v217, 0xffff0000, v118\n"
    "v_mfma_f32_16x16x32_bf16 a[96:99], a[136:139], v[162:165], 0\n"
    "v_lshlrev_b32 v218, 16, v119\n"
    "v_and_b32 v219, 0xffff0000, v119\n"
    "v_mfma_f32_16x16x32_bf16 a[112:115], a[140:143], v[162:165], 0\n"
    "v_add_f32 v96, v216, v178\n"
    "v_add_f32 v97, v217, v179\n"
    "v_mfma_f32_16x16x32_bf16 a[68:71], a[128:131], v[166:169], 0\n"
    "v_add_f32 v98, v218, v180\n"
    "v_add_f32 v99, v219, v181\n"
    "v_mfma_f32_16x16x32_bf16 a[84:87], a[132:135], v[166:169], 0\n"
    "v_fma_f32 v216, v216, v126, -v127\n"
    "v_fma_f32 v217, v217, v126, -v127\n"
    "v_mfma_f32_16x16x32_bf16 a[100:103], a[136:139], v[166:169], 0\n"
    "v_fma_f32 v218, v218, v126, -v127\n"
    "v_fma_f32 v219, v219, v126, -v127\n"
    "v_mfma_f32_16x16x32_bf16 a[116:119], a[140:143], v[166:169], 0\n"
    "v_fmac_f32 v96, v216, v100\n"
    "v_fmac_f32 v97, v217, v101\n"
    "v_mfma_f32_16x16x32_bf16 a[72:75], a[128:131], v[170:173], 0\n"
    "v_fmac_f32 v98, v218, v102\n"
    "v_fmac_f32 v99, v219, v103\n"
    "v_mfma_f32_16x16x32_bf16 a[88:91], a[132:135], v[170:173], 0\n"
    "v_mfma_f32_16x16x32_bf16 a[104:107], a[136:139], v[170:173], 0\n"
    "v_mfma_f32_16x16x32_bf16 a[120:123], a[140:143], v[170:173], 0\n"
    "v_mfma_f32_16x16x32_bf16 a[76:79], a[128:131], v[174:177], 0\n"
    "v_mfma_f32_16x16x32_bf16 a[92:95], a[132:135], v[174:177], 0\n"
    "v_mfma_f32_16x16x32_bf16 a[108:111], a[136:139], v[174:177], 0\n"
    "v_mfma_f32_16x16x32_bf16 a[124:127], a[140:143], v[174:177], 0\n"
    "s_waitcnt vmcnt(9)\n"
    "s_waitcnt vmcnt(0)\n"
    "v_cmp_ne_u64 vcc, v[136:137], s[56:57]\n"
    "s_cbranch_vccnz .Lfsr7_%=\n"
    ".Lfsb8_%=:\n"
    "v_mfma_f32_16x16x32_bf16 a[64:67], a[144:147], v[184:187], a[64:67]\n"
    "s_add_u32 s46, s53, 0\n"
    "v_mfma_f32_16x16x32_bf16 a[80:83], a[148:151], v[184:187], a[80:83]\n"
    "buffer_load_dwordx4 v[146:149], v3, s[16:19], s46 offen offset:0 sc1\n"
    "v_mfma_f32_16x16x32_bf16 a[96:99], a[152:155], v[184:187], a[96:99]\n"
    "s_add_u32 s46, s53, 4096\n"
    "v_mfma_f32_16x16x32_bf16 a[112:115], a[156:159], v[184:187], a[112:115]\n"
    "buffer_load_dwordx4 v[162:165], v3, s[16:19], s46 offen offset:0 sc1\n"
    "v_mfma_f32_16x16x32_bf16 a[68:71], a[144:147], v[188:191], a[68:71]\n"
    "s_add_u32 s46, s53, 8192\n"
    "v_mfma_f32_16x16x32_bf16 a[84:87], a[148:151], v[188:191], a[84:87]\n"
    "buffer_load_dwordx4 v[184:187], v3, s[16:19], s46 offen offset:0 sc1\n"
    "v_mfma_f32_16x16x32_bf16 a[100:103], a[152:155], v[188:191], a[100:103]\n"
    "v_mfma_f32_16x16x32_bf16 a[116:119], a[156:159], v[188:191], a[116:119]\n"
    "v_mfma_f32_16x16x32_bf16 a[72:75], a[144:147], v[192:195], a[72:75]\n"
    "v_mfma_f32_16x16x32_bf16 a[88:91], a[148:151], v[192:195], a[88:91]\n"
    "v_mfma_f32_16x16x32_bf16 a[104:107], a[152:155], v[192:195], a[104:107]\n"
    "v_mfma_f32_16x16x32_bf16 a[120:123], a[156:159], v[192:195], a[120:123]\n"
    "v_mfma_f32_16x16x32_bf16 a[76:79], a[144:147], v[196:199], a[76:79]\n"
    "v_mfma_f32_16x16x32_bf16 a[92:95], a[148:151], v[196:199], a[92:95]\n"
    "v_mfma_f32_16x16x32_bf16 a[108:111], a[152:155], v[196:199], a[108:111]\n"
    "v_mfma_f32_16x16x32_bf16 a[124:127], a[156:159], v[196:199], a[124:127]\n"
    "s_waitcnt vmcnt(8)\n"
    "v_mfma_f32_16x16x32_bf16 a[64:67], a[160:163], v[200:203], a[64:67]\n"
    "s_waitcnt vmcnt(63)\n"
    "ds_read_b128 a[240:243], v108 offset:16384\n"
    "v_mfma_f32_16x16x32_bf16 a[80:83], a[164:167], v[200:203], a[80:83]\n"
    "s_add_u32 s46, s53, 12288\n"
    "ds_read_b128 a[244:247], v108 offset:18432\n"
    "v_mfma_f32_16x16x32_bf16 a[96:99], a[168:171], v[200:203], a[96:99]\n"
    "ds_read_b128 a[248:251], v108 offset:0\n"
    "v_mfma_f32_16x16x32_bf16 a[112:115], a[172:175], v[200:203], a[112:115]\n"
    "buffer_load_dwordx4 v[200:203], v3, s[16:19], s46 offen offset:0 sc1\n"
    "v_mfma_f32_16x16x32_bf16 a[68:71], a[160:163], v[204:207], a[68:71]\n"
    "s_add_u32 s46, s53, 0\n"
    "ds_read_b128 a[252:255], v108 offset:2048\n"
    "v_mfma_f32_16x16x32_bf16 a[84:87], a[164:167], v[204:207], a[84:87]\n"
    "buffer_load_dwordx4 v[150:153], v3, s[16:19], s46 offen offset:1024 sc1\n"
    "v_mfma_f32_16x16x32_bf16 a[100:103], a[168:171], v[204:207], a[100:103]\n"
    "s_add_u32 s46, s53, 4096\n"
    "ds_read_b128 v[10:13], v109 offset:16384\n"
    "v_mfma_f32_16x16x32_bf16 a[116:119], a[172:175], v[204:207], a[116:119]\n"
    "buffer_load_dwordx4 v[166:169], v3, s[16:19], s46 offen offset:1024 sc1\n"
    "v_mfma_f32_16x16x32_bf16 a[72:75], a[160:163], v[208:211], a[72:75]\n"
    "s_add_u32 s46, s53, 8192\n"
    "ds_read_b128 v[14:17], v109 offset:18432\n"
    "v_mfma_f32_16x16x32_bf16 a[88:91], a[164:167], v[208:211], a[88:91]\n"
    "buffer_load_dwordx4 v[188:191], v3, s[16:19], s46 offen offset:1024 sc1\n"
    "v_mfma_f32_16x16x32_bf16 a[104:107], a[168:171], v[208:211], a[104:107]\n"
    "s_add_u32 s46, s53, 12288\n"
    "ds_read_b128 v[18:21], v109 offset:0\n"
    "v_mfma_f32_16x16x32_bf16 a[120:123], a[172:175], v[208:211], a[120:123]\n"
    "buffer_load_dwordx4 v[204:207], v3, s[16:19], s46 offen offset:1024 sc1\n"
    "v_mfma_f32_16x16x32_bf16 a[76:79], a[160:163], v[212:215], a[76:79]\n"
    "s_add_u32 s46, s53, 0\n"
    "ds_read_b128 v[22:25], v109 offset:2048\n"
    "v_mfma_f32_16x16x32_bf16 a[92:95], a[164:167], v[212:215], a[92:95]\n"
    "buffer_load_dwordx4 v[154:157], v3, s[16:19], s46 offen offset:2048 sc1\n"
    "v_mfma_f32_16x16x32_bf16 a[108:111], a[168:171], v[212:215], a[108:111]\n"
    "s_add_u32 s46, s53, 4096\n"
    "v_mfma_f32_16x16x32_bf16 a[124:127], a[172:175], v[212:215], a[124:127]\n"
    "buffer_load_dwordx4 v[170:173], v3, s[16:19], s46 offen offset:2048 sc1\n"
    "s_waitcnt vmcnt(11)\n"
    "v_mfma_f32_16x16x32_bf16 a[64:67], a[176:179], v[88:91], a[64:67]\n"
    "s_add_u32 s46, s53, 8192\n"
    "ds_read_b128 v[26:29], v110 offset:16384\n"
    "v_mfma_f32_16x16x32_bf16 a[80:83], a[180:183], v[88:91], a[80:83]\n"
    "buffer_load_dwordx4 v[192:195], v3, s[16:19], s46 offen offset:2048 sc1\n"
    "v_mfma_f32_16x16x32_bf16 a[96:99], a[184:187], v[88:91], a[96:99]\n"
    "s_add_u32 s46, s53, 12288\n"
    "ds_read_b128 v[34:37], v110 offset:18432\n"
    "v_mfma_f32_16x16x32_bf16 a[112:115], a[188:191], v[88:91], a[112:115]\n"
    "buffer_load_dwordx4 v[208:211], v3, s[16:19], s46 offen offset:2048 sc1\n"
    "v_mfma_f32_16x16x32_bf16 a[68:71], a[176:179], v[104:107], a[68:71]\n"
    "s_add_u32 s46, s53, 0\n"
    "ds_read_b128 v[38:41], v110 offset:0\n"
    "v_mfma_f32_16x16x32_bf16 a[84:87], a[180:183], v[104:107], a[84:87]\n"
    "buffer_load_dwordx4 v[158:161], v3, s[16:19], s46 offen offset:3072 sc1\n"
    "v_mfma_f32_16x16x32_bf16 a[100:103], a[184:187], v[104:107], a[100:103]\n"
    "s_add_u32 s46, s53, 4096\n"
    "ds_read_b128 v[42:45], v110 offset:2048\n"
    "v_mfma_f32_16x16x32_bf16 a[116:119], a[188:191], v[104:107], a[116:119]\n"
    "buffer_load_dwordx4 v[174:177], v3, s[16:19], s46 offen offset:3072 sc1\n"
    "v_mfma_f32_16x16x32_bf16 a[72:75], a[176:179], v[138:141], a[72:75]\n"
    "s_add_u32 s46, s53, 8192\n"
    "ds_read_b128 v[48:51], v111 offset:16384\n"
    "v_mfma_f32_16x16x32_bf16 a[88:91], a[180:183], v[138:141], a[88:91]\n"
    "buffer_load_dwordx4 v[196:199], v3, s[16:19], s46 offen offset:3072 sc1\n"
    "v_mfma_f32_16x16x32_bf16 a[104:107], a[184:187], v[138:141], a[104:107]\n"
    "s_add_u32 s46, s53, 12288\n"
    "ds_read_b128 v[52:55], v111 offset:18432\n"
    "v_mfma_f32_16x16x32_bf16 a[120:123], a[188:191], v[138:141], a[120:123]\n"
    "buffer_load_dwordx4 v[212:215], v3, s[16:19], s46 offen offset:3072 sc1\n"
    "v_mfma_f32_16x16x32_bf16 a[76:79], a[176:179], v[142:145], a[76:79]\n"
    "s_add_u32 s46, s52, 16384\n"
    "ds_read_b128 v[70:73], v111 offset:0\n"
    "v_mfma_f32_16x16x32_bf16 a[92:95], a[180:183], v[142:145], a[92:95]\n"
    "buffer_load_dwordx4 v[88:91], v3, s[16:19], s46 offen offset:0 sc1\n"
    "v_mfma_f32_16x16x32_bf16 a[108:111], a[184:187], v[142:145], a[108:111]\n"
    "ds_read_b128 v[74:77], v111 offset:2048\n"
    "v_mfma_f32_16x16x32_bf16 a[124:127], a[188:191], v[142:145], a[124:127]\n"
    "buffer_load_dwordx4 v[104:107], v3, s[16:19], s46 offen offset:1024 sc1\n"
    "s_waitcnt vmcnt(14) lgkmcnt(0)\n"
    "v_mfma_f32_16x16x32_bf16 a[0:3], a[240:243], v[146:149], a[0:3]\n"
    "v_mfma_f32_16x16x32_bf16 a[16:19], a[244:247], v[146:149], a[16:19]\n"
    "buffer_load_dwordx4 v[138:141], v3, s[16:19], s46 offen offset:2048 sc1\n"
    "v_mfma_f32_16x16x32_bf16 a[32:35], a[248:251], v[146:149], a[32:35]\n"
    "v_mfma_f32_16x16x32_bf16 a[48:51], a[252:255], v[146:149], a[48:51]\n"
    "buffer_load_dwordx4 v[142:145], v3, s[16:19], s46 offen offset:3072 sc1\n"
    "v_mfma_f32_16x16x32_bf16 a[0:3], v[10:13], v[162:165], a[0:3]\n"
    "s_add_u32 s46, s52, 20480\n"
    "v_mfma_f32_16x16x32_bf16 a[16:19], v[14:17], v[162:165], a[16:19]\n"
    "buffer_load_dwordx4 v[146:149], v3, s[16:19], s46 offen offset:0 sc1\n"
    "v_mfma_f32_16x16x32_bf16 a[32:35], v[18:21], v[162:165], a[32:35]\n"
    "v_mfma_f32_16x16x32_bf16 a[48:51], v[22:25], v[162:165], a[48:51]\n"
    "v_mfma_f32_16x16x32_bf16 a[0:3], v[26:29], v[184:187], a[0:3]\n"
    "v_mfma_f32_16x16x32_bf16 a[16:19], v[34:37], v[184:187], a[16:19]\n"
    "v_mfma_f32_16x16x32_bf16 a[32:35], v[38:41], v[184:187], a[32:35]\n"
    "v_mfma_f32_16x16x32_bf16 a[48:51], v[42:45], v[184:187], a[48:51]\n"
    "v_mfma_f32_16x16x32_bf16 a[0:3], v[48:51], v[200:203], a[0:3]\n"
    "v_mfma_f32_16x16x32_bf16 a[16:19], v[52:55], v[200:203], a[16:19]\n"
    "v_mfma_f32_16x16x32_bf16 a[32:35], v[70:73], v[200:203], a[32:35]\n"
    "v_mfma_f32_16x16x32_bf16 a[48:51], v[74:77], v[200:203], a[48:51]\n"
    "s_waitcnt vmcnt(13)\n"
    "v_mfma_f32_16x16x32_bf16 a[4:7], a[240:243], v[150:153], a[4:7]\n"
    "v_mfma_f32_16x16x32_bf16 a[20:23], a[244:247], v[150:153], a[20:23]\n"
    "v_mfma_f32_16x16x32_bf16 a[36:39], a[248:251], v[150:153], a[36:39]\n"
    "s_nop 0\n"
    "ds_write_b128 v33, a[0:3] offset:0\n"
    "v_mfma_f32_16x16x32_bf16 a[52:55], a[252:255], v[150:153], a[52:55]\n"
    "buffer_load_dwordx4 v[150:153], v3, s[16:19], s46 offen offset:1024 sc1\n"
    "v_mfma_f32_16x16x32_bf16 a[4:7], v[10:13], v[166:169], a[4:7]\n"
    "v_mfma_f32_16x16x32_bf16 a[20:23], v[14:17], v[166:169], a[20:23]\n"
    "v_mfma_f32_16x16x32_bf16 a[36:39], v[18:21], v[166:169], a[36:39]\n"
    "ds_write_b128 v46, a[16:19] offset:0\n"
    "v_mfma_f32_16x16x32_bf16 a[52:55], v[22:25], v[166:169], a[52:55]\n"
    "v_mfma_f32_16x16x32_bf16 a[4:7], v[26:29], v[188:191], a[4:7]\n"
    "v_mfma_f32_16x16x32_bf16 a[20:23], v[34:37], v[188:191], a[20:23]\n"
    "v_mfma_f32_16x16x32_bf16 a[36:39], v[38:41], v[188:191], a[36:39]\n"
    "ds_write_b128 v33, a[32:35] offset:128\n"
    "v_mfma_f32_16x16x32_bf16 a[52:55], v[42:45], v[188:191], a[52:55]\n"
    "v_mfma_f32_16x16x32_bf16 a[4:7], v[48:51], v[204:207], a[4:7]\n"
    "v_mfma_f32_16x16x32_bf16 a[20:23], v[52:55], v[204:207], a[20:23]\n"
    "v_mfma_f32_16x16x32_bf16 a[36:39], v[70:73], v[204:207], a[36:39]\n"
    "ds_write_b128 v46, a[48:51] offset:128\n"
    "v_mfma_f32_16x16x32_bf16 a[52:55], v[74:77], v[204:207], a[52:55]\n"
    "s_waitcnt vmcnt(10)\n"
    "v_mfma_f32_16x16x32_bf16 a[8:11], a[240:243], v[154:157], a[8:11]\n"
    "v_mfma_f32_16x16x32_bf16 a[24:27], a[244:247], v[154:157], a[24:27]\n"
    "v_mfma_f32_16x16x32_bf16 a[40:43], a[248:251], v[154:157], a[40:43]\n"
    "ds_write_b128 v33, a[4:7] offset:1024\n"
    "v_mfma_f32_16x16x32_bf16 a[56:59], a[252:255], v[154:157], a[56:59]\n"
    "buffer_load_dwordx4 v[154:157], v3, s[16:19], s46 offen offset:2048 sc1\n"
    "v_mfma_f32_16x16x32_bf16 a[8:11], v[10:13], v[170:173], a[8:11]\n"
    "v_mfma_f32_16x16x32_bf16 a[24:27], v[14:17], v[170:173], a[24:27]\n"
    "v_mfma_f32_16x16x32_bf16 a[40:43], v[18:21], v[170:173], a[40:43]\n"
    "ds_write_b128 v46, a[20:23] offset:1024\n"
    "v_mfma_f32_16x16x32_bf16 a[56:59], v[22:25], v[170:173], a[56:59]\n"
    "v_mfma_f32_16x16x32_bf16 a[8:11], v[26:29], v[192:195], a[8:11]\n"
    "v_mfma_f32_16x16x32_bf16 a[24:27], v[34:37], v[192:195], a[24:27]\n"
    "v_mfma_f32_16x16x32_bf16 a[40:43], v[38:41], v[192:195], a[40:43]\n"
    "ds_write_b128 v33, a[36:39] offset:1152\n"
    "v_mfma_f32_16x16x32_bf16 a[56:59], v[42:45], v[192:195], a[56:59]\n"
    "v_mfma_f32_16x16x32_bf16 a[8:11], v[48:51], v[208:211], a[8:11]\n"
    "v_mfma_f32_16x16x32_bf16 a[24:27], v[52:55], v[208:211], a[24:27]\n"
    "v_mfma_f32_16x16x32_bf16 a[40:43], v[70:73], v[208:211], a[40:43]\n"
    "ds_write_b128 v46, a[52:55] offset:1152\n"
    "v_mfma_f32_16x16x32_bf16 a[56:59], v[74:77], v[208:211], a[56:59]\n"
    "s_waitcnt vmcnt(7)\n"
    "v_mfma_f32_16x16x32_bf16 a[12:15], a[240:243], v[158:161], a[12:15]\n"
    "v_mfma_f32_16x16x32_bf16 a[28:31], a[244:247], v[158:161], a[28:31]\n"
    "v_mfma_f32_16x16x32_bf16 a[44:47], a[248:251], v[158:161], a[44:47]\n"
    "ds_write_b128 v33, a[8:11] offset:2048\n"
    "v_mfma_f32_16x16x32_bf16 a[60:63], a[252:255], v[158:161], a[60:63]\n"
    "buffer_load_dwordx4 v[158:161], v3, s[16:19], s46 offen offset:3072 sc1\n"
    "v_mfma_f32_16x16x32_bf16 a[12:15], v[10:13], v[174:177], a[12:15]\n"
    "s_add_u32 s46, s52, 24576\n"
    "v_mfma_f32_16x16x32_bf16 a[28:31], v[14:17], v[174:177], a[28:31]\n"
    "buffer_load_dwordx4 v[162:165], v3, s[16:19], s46 offen offset:0 sc1\n"
    "v_mfma_f32_16x16x32_bf16 a[44:47], v[18:21], v[174:177], a[44:47]\n"
    "ds_write_b128 v46, a[24:27] offset:2048\n"
    "v_mfma_f32_16x16x32_bf16 a[60:63], v[22:25], v[174:177], a[60:63]\n"
    "buffer_load_dwordx4 v[166:169], v3, s[16:19], s46 offen offset:1024 sc1\n"
    "v_mfma_f32_16x16x32_bf16 a[12:15], v[26:29], v[196:199], a[12:15]\n"
    "v_mfma_f32_16x16x32_bf16 a[28:31], v[34:37], v[196:199], a[28:31]\n"
    "buffer_load_dwordx4 v[170:173], v3, s[16:19], s46 offen offset:2048 sc1\n"
    "v_mfma_f32_16x16x32_bf16 a[44:47], v[38:41], v[196:199], a[44:47]\n"
    "ds_write_b128 v33, a[40:43] offset:2176\n"
    "v_mfma_f32_16x16x32_bf16 a[60:63], v[42:45], v[196:199], a[60:63]\n"
    "buffer_load_dwordx4 v[174:177], v3, s[16:19], s46 offen offset:3072 sc1\n"
    "v_mfma_f32_16x16x32_bf16 a[12:15], v[48:51], v[212:215], a[12:15]\n"
    "s_add_u32 s46, s54, 0\n"
    "v_mfma_f32_16x16x32_bf16 a[28:31], v[52:55], v[212:215], a[28:31]\n"
    "buffer_load_dwordx4 v[184:187], v3, s[16:19], s46 offen offset:0 sc1\n"
    "v_mfma_f32_16x16x32_bf16 a[44:47], v[70:73], v[212:215], a[44:47]\n"
    "s_add_u32 s46, s54, 4096\n"
    "ds_write_b128 v46, a[56:59] offset:2176\n"
    "v_mfma_f32_16x16x32_bf16 a[60:63], v[74:77], v[212:215], a[60:63]\n"
    "buffer_load_dwordx4 v[200:203], v3, s[16:19], s46 offen offset:0 sc1\n"
    "ds_write_b128 v33, a[12:15] offset:3072\n"
    "s_nop 0\n"
    "ds_write_b128 v46, a[28:31] offset:3072\n"
    "s_nop 0\n"
    "ds_write_b128 v33, a[44:47] offset:3200\n"
    "s_nop 1\n"
    "ds_write_b128 v46, a[60:63] offset:3200\n"
    "s_waitcnt lgkmcnt(0)\n"
    "s_barrier\n"
    "s_waitcnt vmcnt(10)\n"
    "v_mfma_f32_16x16x32_bf16 a[64:67], a[192:195], v[88:91], a[64:67]\n"
    "ds_read_b128 v[220:223], v59 offset:0\n"
    "v_mfma_f32_16x16x32_bf16 a[80:83], a[196:199], v[88:91], a[80:83]\n"
    "s_add_u32 s46, s54, 8192\n"
    "ds_read_b128 v[112:115], v59 offset:4096\n"
    "v_mfma_f32_16x16x32_bf16 a[96:99], a[200:203], v[88:91], a[96:99]\n"
    "ds_read_b128 v[116:119], v59 offset:8192\n"
    "v_mfma_f32_16x16x32_bf16 a[112:115], a[204:207], v[88:91], a[112:115]\n"
    "buffer_load_dwordx4 v[88:91], v3, s[16:19], s46 offen offset:0 sc1\n"
    "v_mfma_f32_16x16x32_bf16 a[68:71], a[192:195], v[104:107], a[68:71]\n"
    "ds_read_b128 v[120:123], v59 offset:12288\n"
    "v_mfma_f32_16x16x32_bf16 a[84:87], a[196:199], v[104:107], a[84:87]\n"
    "ds_read_b128 v[224:227], v59 offset:1024\n"
    "v_mfma_f32_16x16x32_bf16 a[100:103], a[200:203], v[104:107], a[100:103]\n"
    "ds_read_b128 v[216:219], v59 offset:5120\n"
    "v_mfma_f32_16x16x32_bf16 a[116:119], a[204:207], v[104:107], a[116:119]\n"
    "ds_read_b128 v[100:103], v59 offset:9216\n"
    "v_mfma_f32_16x16x32_bf16 a[72:75], a[192:195], v[138:141], a[72:75]\n"
    "ds_read_b128 v[178:181], v59 offset:13312\n"
    "s_waitcnt lgkmcnt(4)\n"
    "v_mfma_f32_16x16x32_bf16 a[88:91], a[196:199], v[138:141], a[88:91]\n"
    "v_add_f32 v220, v220, v112\n"
    "v_add_f32 v221, v221, v113\n"
    "v_mfma_f32_16x16x32_bf16 a[104:107], a[200:203], v[138:141], a[104:107]\n"
    "v_add_f32 v222, v222, v114\n"
    "v_add_f32 v223, v223, v115\n"
    "v_mfma_f32_16x16x32_bf16 a[120:123], a[204:207], v[138:141], a[120:123]\n"
    "v_add_f32 v116, v116, v120\n"
    "v_add_f32 v117, v117, v121\n"
    "v_mfma_f32_16x16x32_bf16 a[76:79], a[192:195], v[142:145], a[76:79]\n"
    "v_add_f32 v118, v118, v122\n"
    "v_add_f32 v119, v119, v123\n"
    "v_mfma_f32_16x16x32_bf16 a[92:95], a[196:199], v[142:145], a[92:95]\n"
    "v_add_f32 v220, v220, v116\n"
    "v_add_f32 v221, v221, v117\n"
    "v_mfma_f32_16x16x32_bf16 a[108:111], a[200:203], v[142:145], a[108:111]\n"
    "v_add_f32 v222, v222, v118\n"
    "v_add_f32 v223, v223, v119\n"
    "v_mfma_f32_16x16x32_bf16 a[124:127], a[204:207], v[142:145], a[124:127]\n"
    "ds_read_b128 v[228:231], v59 offset:2048\n"
    "s_waitcnt vmcnt(7)\n"
    "v_mfma_f32_16x16x32_bf16 a[64:67], a[208:211], v[146:149], a[64:67]\n"
    "ds_read_b128 v[112:115], v59 offset:6144\n"
    "v_mfma_f32_16x16x32_bf16 a[80:83], a[212:215], v[146:149], a[80:83]\n"
    "s_add_u32 s46, s54, 12288\n"
    "ds_read_b128 v[116:119], v59 offset:10240\n"
    "v_mfma_f32_16x16x32_bf16 a[96:99], a[216:219], v[146:149], a[96:99]\n"
    "ds_read_b128 v[120:123], v59 offset:14336\n"
    "v_cvt_pk_bf16_f32 v220, v220, v221\n"
    "v_mfma_f32_16x16x32_bf16 a[112:115], a[220:223], v[146:149], a[112:115]\n"
    "buffer_load_dwordx4 v[146:149], v3, s[16:19], s46 offen offset:0 sc1\n"
    "v_mfma_f32_16x16x32_bf16 a[68:71], a[208:211], v[150:153], a[68:71]\n"
    "s_add_u32 s46, s54, 0\n"
    "v_cvt_pk_bf16_f32 v221, v222, v223\n"
    "v_mov_b32 v222, s56\n"
    "v_mfma_f32_16x16x32_bf16 a[84:87], a[212:215], v[150:153], a[84:87]\n"
    "buffer_load_dwordx4 v[188:191], v3, s[16:19], s46 offen offset:1024 sc1\n"
    "v_mfma_f32_16x16x32_bf16 a[100:103], a[216:219], v[150:153], a[100:103]\n"
    "s_add_u32 s46, s54, 4096\n"
    "v_mov_b32 v223, s57\n"
    "v_mfma_f32_16x16x32_bf16 a[116:119], a[220:223], v[150:153], a[116:119]\n"
    "buffer_load_dwordx4 v[204:207], v3, s[16:19], s46 offen offset:1024 sc1\n"
    "v_mfma_f32_16x16x32_bf16 a[72:75], a[208:211], v[154:157], a[72:75]\n"
    "buffer_store_dwordx4 v[220:223], v3, s[16:19], s20 offen offset:0 sc1\n"
    "v_mfma_f32_16x16x32_bf16 a[88:91], a[212:215], v[154:157], a[88:91]\n"
    "s_add_u32 s46, s54, 8192\n"
    "s_waitcnt lgkmcnt(4)\n"
    "v_add_f32 v224, v224, v216\n"
    "v_mfma_f32_16x16x32_bf16 a[104:107], a[216:219], v[154:157], a[104:107]\n"
    "buffer_load_dwordx4 v[104:107], v3, s[16:19], s46 offen offset:1024 sc1\n"
    "v_mfma_f32_16x16x32_bf16 a[120:123], a[220:223], v[154:157], a[120:123]\n"
    "s_add_u32 s46, s54, 12288\n"
    "v_add_f32 v225, v225, v217\n"
    "v_add_f32 v226, v226, v218\n"
    "v_mfma_f32_16x16x32_bf16 a[76:79], a[208:211], v[158:161], a[76:79]\n"
    "buffer_load_dwordx4 v[150:153], v3, s[16:19], s46 offen offset:1024 sc1\n"
    "v_mfma_f32_16x16x32_bf16 a[92:95], a[212:215], v[158:161], a[92:95]\n"
    "s_add_u32 s46, s54, 0\n"
    "v_add_f32 v227, v227, v219\n"
    "v_add_f32 v100, v100, v178\n"
    "v_mfma_f32_16x16x32_bf16 a[108:111], a[216:219], v[158:161], a[108:111]\n"
    "buffer_load_dwordx4 v[192:195], v3, s[16:19], s46 offen offset:2048 sc1\n"
    "v_mfma_f32_16x16x32_bf16 a[124:127], a[220:223], v[158:161], a[124:127]\n"
    "s_add_u32 s46, s54, 4096\n"
    "v_add_f32 v101, v101, v179\n"
    "v_add_f32 v102, v102, v180\n"
    "buffer_load_dwordx4 v[208:211], v3, s[16:19], s46 offen offset:2048 sc1\n"
    "s_waitcnt vmcnt(11)\n"
    "v_add_f32 v103, v103, v181\n"
    "v_add_f32 v224, v224, v100\n"
    "v_add_f32 v225, v225, v101\n"
    "v_add_f32 v226, v226, v102\n"
    "v_add_f32 v227, v227, v103\n"
    "ds_read_b128 v[232:235], v59 offset:3072\n"
    "ds_read_b128 v[216:219], v59 offset:7168\n"
    "ds_read_b128 v[100:103], v59 offset:11264\n"
    "ds_read_b128 v[178:181], v59 offset:15360\n"
    "v_cvt_pk_bf16_f32 v224, v224, v225\n"
    "v_cvt_pk_bf16_f32 v225, v226, v227\n"
    "v_mov_b32 v226, s56\n"
    "v_mov_b32 v227, s57\n"
    "buffer_store_dwordx4 v[224:227], v3, s[16:19], s20 offen offset:1024 sc1\n"
    "s_waitcnt lgkmcnt(4)\n"
    "v_add_f32 v228, v228, v112\n"
    "v_add_f32 v229, v229, v113\n"
    "v_add_f32 v230, v230, v114\n"
    "v_add_f32 v231, v231, v115\n"
    "v_add_f32 v116, v116, v120\n"
    "v_add_f32 v117, v117, v121\n"
    "v_add_f32 v118, v118, v122\n"
    "v_add_f32 v119, v119, v123\n"
    "v_add_f32 v228, v228, v116\n"
    "v_add_f32 v229, v229, v117\n"
    "v_add_f32 v230, v230, v118\n"
    "v_add_f32 v231, v231, v119\n"
    "v_cvt_pk_bf16_f32 v228, v228, v229\n"
    "v_cvt_pk_bf16_f32 v229, v230, v231\n"
    "v_mov_b32 v230, s56\n"
    "v_mov_b32 v231, s57\n"
    "buffer_store_dwordx4 v[228:231], v3, s[16:19], s20 offen offset:2048 sc1\n"
    "s_waitcnt lgkmcnt(0)\n"
    "v_add_f32 v232, v232, v216\n"
    "v_add_f32 v233, v233, v217\n"
    "v_add_f32 v234, v234, v218\n"
    "v_add_f32 v235, v235, v219\n"
    "v_add_f32 v100, v100, v178\n"
    "v_add_f32 v101, v101, v179\n"
    "v_add_f32 v102, v102, v180\n"
    "v_add_f32 v103, v103, v181\n"
    "v_add_f32 v232, v232, v100\n"
    "v_add_f32 v233, v233, v101\n"
    "v_add_f32 v234, v234, v102\n"
    "v_add_f32 v235, v235, v103\n"
    "v_cvt_pk_bf16_f32 v232, v232, v233\n"
    "v_cvt_pk_bf16_f32 v233, v234, v235\n"
    "v_mov_b32 v234, s56\n"
    "v_mov_b32 v235, s57\n"
    "buffer_store_dwordx4 v[232:235], v3, s[16:19], s20 offen offset:3072 sc1\n"
    "v_add_u32 v59, s22, v59\n"
    "v_mfma_f32_16x16x32_bf16 a[64:67], a[224:227], v[162:165], a[64:67]\n"
    "s_add_u32 s46, s54, 8192\n"
    "v_mfma_f32_16x16x32_bf16 a[80:83], a[228:231], v[162:165], a[80:83]\n"
    "buffer_load_dwordx4 v[138:141], v3, s[16:19], s46 offen offset:2048 sc1\n"
    "v_mfma_f32_16x16x32_bf16 a[96:99], a[232:235], v[162:165], a[96:99]\n"
    "s_add_u32 s46, s54, 12288\n"
    "v_mfma_f32_16x16x32_bf16 a[112:115], a[236:239], v[162:165], a[112:115]\n"
    "buffer_load_dwordx4 v[154:157], v3, s[16:19], s46 offen offset:2048 sc1\n"
    "v_mfma_f32_16x16x32_bf16 a[68:71], a[224:227], v[166:169], a[68:71]\n"
    "s_add_u32 s46, s54, 0\n"
    "v_mfma_f32_16x16x32_bf16 a[84:87], a[228:231], v[166:169], a[84:87]\n"
    "buffer_load_dwordx4 v[196:199], v3, s[16:19], s46 offen offset:3072 sc1\n"
    "v_mfma_f32_16x16x32_bf16 a[100:103], a[232:235], v[166:169], a[100:103]\n"
    "s_add_u32 s46, s54, 4096\n"
    "v_mfma_f32_16x16x32_bf16 a[116:119], a[236:239], v[166:169], a[116:119]\n"
    "buffer_load_dwordx4 v[212:215], v3, s[16:19], s46 offen offset:3072 sc1\n"
    "v_mfma_f32_16x16x32_bf16 a[72:75], a[224:227], v[170:173], a[72:75]\n"
    "s_add_u32 s46, s54, 8192\n"
    "v_mfma_f32_16x16x32_bf16 a[88:91], a[228:231], v[170:173], a[88:91]\n"
    "buffer_load_dwordx4 v[142:145], v3, s[16:19], s46 offen offset:3072 sc1\n"
    "v_mfma_f32_16x16x32_bf16 a[104:107], a[232:235], v[170:173], a[104:107]\n"
    "s_add_u32 s46, s54, 12288\n"
    "v_mfma_f32_16x16x32_bf16 a[120:123], a[236:239], v[170:173], a[120:123]\n"
    "buffer_load_dwordx4 v[158:161], v3, s[16:19], s46 offen offset:3072 sc1\n"
    "v_mfma_f32_16x16x32_bf16 a[76:79], a[224:227], v[174:177], a[76:79]\n"
    "v_mfma_f32_16x16x32_bf16 a[92:95], a[228:231], v[174:177], a[92:95]\n"
    "v_mfma_f32_16x16x32_bf16 a[108:111], a[232:235], v[174:177], a[108:111]\n"
    "v_mfma_f32_16x16x32_bf16 a[124:127], a[236:239], v[174:177], a[124:127]\n"
    "s_waitcnt vmcnt(16)\n"
    "v_mfma_f32_16x16x32_bf16 a[64:67], a[240:243], v[184:187], a[64:67]\n"
    "v_mfma_f32_16x16x32_bf16 a[80:83], a[244:247], v[184:187], a[80:83]\n"
    "v_mfma_f32_16x16x32_bf16 a[96:99], a[248:251], v[184:187], a[96:99]\n"
    "v_mfma_f32_16x16x32_bf16 a[112:115], a[252:255], v[184:187], a[112:115]\n"
    "v_mfma_f32_16x16x32_bf16 a[64:67], v[10:13], v[200:203], a[64:67]\n"
    "v_mfma_f32_16x16x32_bf16 a[80:83], v[14:17], v[200:203], a[80:83]\n"
    "v_mfma_f32_16x16x32_bf16 a[96:99], v[18:21], v[200:203], a[96:99]\n"
    "v_mfma_f32_16x16x32_bf16 a[112:115], v[22:25], v[200:203], a[112:115]\n"
    "v_mfma_f32_16x16x32_bf16 a[64:67], v[26:29], v[88:91], a[64:67]\n"
    "v_mfma_f32_16x16x32_bf16 a[80:83], v[34:37], v[88:91], a[80:83]\n"
    "v_mfma_f32_16x16x32_bf16 a[96:99], v[38:41], v[88:91], a[96:99]\n"
    "v_mfma_f32_16x16x32_bf16 a[112:115], v[42:45], v[88:91], a[112:115]\n"
    "v_mfma_f32_16x16x32_bf16 a[64:67], v[48:51], v[146:149], a[64:67]\n"
    "v_mfma_f32_16x16x32_bf16 a[80:83], v[52:55], v[146:149], a[80:83]\n"
    "v_mfma_f32_16x16x32_bf16 a[96:99], v[70:73], v[146:149], a[96:99]\n"
    "v_mfma_f32_16x16x32_bf16 a[112:115], v[74:77], v[146:149], a[112:115]\n"
    "s_waitcnt vmcnt(11)\n"
    "v_mfma_f32_16x16x32_bf16 a[68:71], a[240:243], v[188:191], a[68:71]\n"
    "v_mfma_f32_16x16x32_bf16 a[84:87], a[244:247], v[188:191], a[84:87]\n"
    "v_mfma_f32_16x16x32_bf16 a[100:103], a[248:251], v[188:191], a[100:103]\n"
    "s_nop 0\n"
    "ds_write_b128 v47, a[64:67] offset:0\n"
    "v_mfma_f32_16x16x32_bf16 a[116:119], a[252:255], v[188:191], a[116:119]\n"
    "v_mfma_f32_16x16x32_bf16 a[68:71], v[10:13], v[204:207], a[68:71]\n"
    "v_mfma_f32_16x16x32_bf16 a[84:87], v[14:17], v[204:207], a[84:87]\n"
    "v_mfma_f32_16x16x32_bf16 a[100:103], v[18:21], v[204:207], a[100:103]\n"
    "ds_write_b128 v58, a[80:83] offset:0\n"
    "v_mfma_f32_16x16x32_bf16 a[116:119], v[22:25], v[204:207], a[116:119]\n"
    "v_mfma_f32_16x16x32_bf16 a[68:71], v[26:29], v[104:107], a[68:71]\n"
    "v_mfma_f32_16x16x32_bf16 a[84:87], v[34:37], v[104:107], a[84:87]\n"
    "v_mfma_f32_16x16x32_bf16 a[100:103], v[38:41], v[104:107], a[100:103]\n"
    "ds_write_b128 v47, a[96:99] offset:128\n"
    "v_mfma_f32_16x16x32_bf16 a[116:119], v[42:45], v[104:107], a[116:119]\n"
    "v_mfma_f32_16x16x32_bf16 a[68:71], v[48:51], v[150:153], a[68:71]\n"
    "v_mfma_f32_16x16x32_bf16 a[84:87], v[52:55], v[150:153], a[84:87]\n"
    "v_mfma_f32_16x16x32_bf16 a[100:103], v[70:73], v[150:153], a[100:103]\n"
    "ds_write_b128 v58, a[112:115] offset:128\n"
    "v_mfma_f32_16x16x32_bf16 a[116:119], v[74:77], v[150:153], a[116:119]\n"
    "s_waitcnt vmcnt(4)\n"
    "v_mfma_f32_16x16x32_bf16 a[72:75], a[240:243], v[192:195], a[72:75]\n"
    "v_mfma_f32_16x16x32_bf16 a[88:91], a[244:247], v[192:195], a[88:91]\n"
    "buffer_load_dwordx4 v[220:223], v3, s[16:19], s21 offen offset:0 sc1\n"
    "v_mfma_f32_16x16x32_bf16 a[104:107], a[248:251], v[192:195], a[104:107]\n"
    "ds_write_b128 v47, a[68:71] offset:1024\n"
    "v_mfma_f32_16x16x32_bf16 a[120:123], a[252:255], v[192:195], a[120:123]\n"
    "v_mfma_f32_16x16x32_bf16 a[72:75], v[10:13], v[208:211], a[72:75]\n"
    "v_mfma_f32_16x16x32_bf16 a[88:91], v[14:17], v[208:211], a[88:91]\n"
    "buffer_load_dwordx4 v[224:227], v3, s[16:19], s21 offen offset:1024 sc1\n"
    "v_mfma_f32_16x16x32_bf16 a[104:107], v[18:21], v[208:211], a[104:107]\n"
    "ds_write_b128 v58, a[84:87] offset:1024\n"
    "v_mfma_f32_16x16x32_bf16 a[120:123], v[22:25], v[208:211], a[120:123]\n"
    "v_mfma_f32_16x16x32_bf16 a[72:75], v[26:29], v[138:141], a[72:75]\n"
    "v_mfma_f32_16x16x32_bf16 a[88:91], v[34:37], v[138:141], a[88:91]\n"
    "buffer_load_dwordx4 v[228:231], v3, s[16:19], s21 offen offset:2048 sc1\n"
    "v_mfma_f32_16x16x32_bf16 a[104:107], v[38:41], v[138:141], a[104:107]\n"
    "ds_write_b128 v47, a[100:103] offset:1152\n"
    "v_mfma_f32_16x16x32_bf16 a[120:123], v[42:45], v[138:141], a[120:123]\n"
    "v_mfma_f32_16x16x32_bf16 a[72:75], v[48:51], v[154:157], a[72:75]\n"
    "v_mfma_f32_16x16x32_bf16 a[88:91], v[52:55], v[154:157], a[88:91]\n"
    "buffer_load_dwordx4 v[232:235], v3, s[16:19], s21 offen offset:3072 sc1\n"
    "v_mfma_f32_16x16x32_bf16 a[104:107], v[70:73], v[154:157], a[104:107]\n"
    "ds_write_b128 v58, a[116:119] offset:1152\n"
    "v_mfma_f32_16x16x32_bf16 a[120:123], v[74:77], v[154:157], a[120:123]\n"
    "s_waitcnt vmcnt(4)\n"
    "v_mfma_f32_16x16x32_bf16 a[76:79], a[240:243], v[196:199], a[76:79]\n"
    "v_mfma_f32_16x16x32_bf16 a[92:95], a[244:247], v[196:199], a[92:95]\n"
    "v_mfma_f32_16x16x32_bf16 a[108:111], a[248:251], v[196:199], a[108:111]\n"
    "ds_write_b128 v47, a[72:75] offset:2048\n"
    "v_mfma_f32_16x16x32_bf16 a[124:127], a[252:255], v[196:199], a[124:127]\n"
    "v_mfma_f32_16x16x32_bf16 a[76:79], v[10:13], v[212:215], a[76:79]\n"
    "v_mfma_f32_16x16x32_bf16 a[92:95], v[14:17], v[212:215], a[92:95]\n"
    "v_mfma_f32_16x16x32_bf16 a[108:111], v[18:21], v[212:215], a[108:111]\n"
    "ds_write_b128 v58, a[88:91] offset:2048\n"
    "v_mfma_f32_16x16x32_bf16 a[124:127], v[22:25], v[212:215], a[124:127]\n"
    "v_mfma_f32_16x16x32_bf16 a[76:79], v[26:29], v[142:145], a[76:79]\n"
    "v_mfma_f32_16x16x32_bf16 a[92:95], v[34:37], v[142:145], a[92:95]\n"
    "v_mfma_f32_16x16x32_bf16 a[108:111], v[38:41], v[142:145], a[108:111]\n"
    "ds_write_b128 v47, a[104:107] offset:2176\n"
    "v_mfma_f32_16x16x32_bf16 a[124:127], v[42:45], v[142:145], a[124:127]\n"
    "v_mfma_f32_16x16x32_bf16 a[76:79], v[48:51], v[158:161], a[76:79]\n"
    "v_mfma_f32_16x16x32_bf16 a[92:95], v[52:55], v[158:161], a[92:95]\n"
    "v_mfma_f32_16x16x32_bf16 a[108:111], v[70:73], v[158:161], a[108:111]\n"
    "ds_write_b128 v58, a[120:123] offset:2176\n"
    "v_mfma_f32_16x16x32_bf16 a[124:127], v[74:77], v[158:161], a[124:127]\n"
    "s_nop 3\n"
    "ds_write_b128 v47, a[76:79] offset:3072\n"
    "ds_write_b128 v58, a[92:95] offset:3072\n"
    "ds_write_b128 v47, a[108:111] offset:3200\n"
    "s_nop 0\n"
    "ds_write_b128 v58, a[124:127] offset:3200\n"
    "s_waitcnt lgkmcnt(0)\n"
    "s_barrier\n"
    "ds_read_b128 v[88:91], v59 offset:0\n"
    "ds_read_b128 v[146:149], v59 offset:4096\n"
    "ds_read_b128 v[162:165], v59 offset:8192\n"
    "ds_read_b128 v[184:187], v59 offset:12288\n"
    "ds_read_b128 v[104:107], v59 offset:1024\n"
    "ds_read_b128 v[150:153], v59 offset:5120\n"
    "ds_read_b128 v[166:169], v59 offset:9216\n"
    "ds_read_b128 v[188:191], v59 offset:13312\n"
    "ds_read_b128 v[138:141], v59 offset:2048\n"
    "ds_read_b128 v[154:157], v59 offset:6144\n"
    "ds_read_b128 v[170:173], v59 offset:10240\n"
    "ds_read_b128 v[192:195], v59 offset:14336\n"
    "ds_read_b128 v[142:145], v59 offset:3072\n"
    "ds_read_b128 v[158:161], v59 offset:7168\n"
    "ds_read_b128 v[174:177], v59 offset:11264\n"
    "ds_read_b128 v[196:199], v59 offset:15360\n"
    "s_waitcnt vmcnt(0)\n"
    "v_cmp_ne_u64 s[74:75], v[222:223], s[56:57]\n"
    "v_cmp_ne_u64 vcc, v[226:227], s[56:57]\n"
    "s_or_b64 s[74:75], s[74:75], vcc\n"
    "v_cmp_ne_u64 vcc, v[230:231], s[56:57]\n"
    "s_or_b64 s[74:75], s[74:75], vcc\n"
    "v_cmp_ne_u64 vcc, v[234:235], s[56:57]\n"
    "s_or_b64 vcc, s[74:75], vcc\n"
    "s_cbranch_vccnz .Lxir9_%=\n"
    ".Lxib10_%=:\n"
    "v_and_b32 v223, 0xffff0000, v221\n"
    "v_lshlrev_b32 v222, 16, v221\n"
    "v_and_b32 v221, 0xffff0000, v220\n"
    "v_lshlrev_b32 v220, 16, v220\n"
    "v_pk_add_f32 v[78:79], v[78:79], v[220:221]\n"
    "v_pk_add_f32 v[80:81], v[80:81], v[222:223]\n"
    "s_waitcnt lgkmcnt(12)\n"
    "v_pk_add_f32 v[88:89], v[88:89], v[146:147]\n"
    "v_pk_add_f32 v[90:91], v[90:91], v[148:149]\n"
    "v_pk_add_f32 v[162:163], v[162:163], v[184:185]\n"
    "v_pk_add_f32 v[164:165], v[164:165], v[186:187]\n"
    "v_pk_add_f32 v[78:79], v[78:79], v[88:89]\n"
    "v_pk_add_f32 v[80:81], v[80:81], v[90:91]\n"
    "v_pk_add_f32 v[78:79], v[78:79], v[162:163]\n"
    "v_pk_add_f32 v[80:81], v[80:81], v[164:165]\n"
    "s_movk_i32 s70, 0\n"
    "buffer_store_dwordx4 v[78:81], v135, s[36:39], s70 offen sc1\n"
    "v_and_b32 v227, 0xffff0000, v225\n"
    "v_lshlrev_b32 v226, 16, v225\n"
    "v_and_b32 v225, 0xffff0000, v224\n"
    "v_lshlrev_b32 v224, 16, v224\n"
    "v_pk_add_f32 v[82:83], v[82:83], v[224:225]\n"
    "v_pk_add_f32 v[84:85], v[84:85], v[226:227]\n"
    "s_waitcnt lgkmcnt(8)\n"
    "v_pk_add_f32 v[104:105], v[104:105], v[150:151]\n"
    "v_pk_add_f32 v[106:107], v[106:107], v[152:153]\n"
    "v_pk_add_f32 v[166:167], v[166:167], v[188:189]\n"
    "v_pk_add_f32 v[168:169], v[168:169], v[190:191]\n"
    "v_pk_add_f32 v[82:83], v[82:83], v[104:105]\n"
    "v_pk_add_f32 v[84:85], v[84:85], v[106:107]\n"
    "v_pk_add_f32 v[82:83], v[82:83], v[166:167]\n"
    "v_pk_add_f32 v[84:85], v[84:85], v[168:169]\n"
    "s_movk_i32 s70, 8192\n"
    "buffer_store_dwordx4 v[82:85], v135, s[36:39], s70 offen sc1\n"
    "v_and_b32 v231, 0xffff0000, v229\n"
    "v_lshlrev_b32 v230, 16, v229\n"
    "v_and_b32 v229, 0xffff0000, v228\n"
    "v_lshlrev_b32 v228, 16, v228\n"
    "v_pk_add_f32 v[92:93], v[92:93], v[228:229]\n"
    "v_pk_add_f32 v[94:95], v[94:95], v[230:231]\n"
    "s_waitcnt lgkmcnt(4)\n"
    "v_pk_add_f32 v[138:139], v[138:139], v[154:155]\n"
    "v_pk_add_f32 v[140:141], v[140:141], v[156:157]\n"
    "v_pk_add_f32 v[170:171], v[170:171], v[192:193]\n"
    "v_pk_add_f32 v[172:173], v[172:173], v[194:195]\n"
    "v_pk_add_f32 v[92:93], v[92:93], v[138:139]\n"
    "v_pk_add_f32 v[94:95], v[94:95], v[140:141]\n"
    "v_pk_add_f32 v[92:93], v[92:93], v[170:171]\n"
    "v_pk_add_f32 v[94:95], v[94:95], v[172:173]\n"
    "s_movk_i32 s70, 16384\n"
    "buffer_store_dwordx4 v[92:95], v135, s[36:39], s70 offen sc1\n"
    "v_and_b32 v235, 0xffff0000, v233\n"
    "v_lshlrev_b32 v234, 16, v233\n"
    "v_and_b32 v233, 0xffff0000, v232\n"
    "v_lshlrev_b32 v232, 16, v232\n"
    "v_pk_add_f32 v[96:97], v[96:97], v[232:233]\n"
    "v_pk_add_f32 v[98:99], v[98:99], v[234:235]\n"
    "s_waitcnt lgkmcnt(0)\n"
    "v_pk_add_f32 v[142:143], v[142:143], v[158:159]\n"
    "v_pk_add_f32 v[144:145], v[144:145], v[160:161]\n"
    "v_pk_add_f32 v[174:175], v[174:175], v[196:197]\n"
    "v_pk_add_f32 v[176:177], v[176:177], v[198:199]\n"
    "v_pk_add_f32 v[96:97], v[96:97], v[142:143]\n"
    "v_pk_add_f32 v[98:99], v[98:99], v[144:145]\n"
    "v_pk_add_f32 v[96:97], v[96:97], v[174:175]\n"
    "v_pk_add_f32 v[98:99], v[98:99], v[176:177]\n"
    "s_movk_i32 s70, 24576\n"
    "buffer_store_dwordx4 v[96:99], v135, s[36:39], s70 offen sc1\n"
    "s_cmp_lg_u32 %1, 199\n"
    "s_cbranch_scc1 .LnoE11_%=\n"
    "s_cmp_lg_u32 s33, 0\n"
    "s_cbranch_scc1 .LnoE11_%=\n"
    "s_mov_b64 exec, 1\n"
    "buffer_store_dwordx2 v[56:57], off, s[16:19], 0 sc1\n"
    "s_mov_b64 exec, -1\n"
    ".LnoE11_%=:\n"
    "s_endpgm\n"
    ".Lstr1_%=:\n"
    "s_sleep 1\n"
    "s_cmp_eq_u32 s72, 0\n"
    "s_cbranch_scc1 .Lstb2_%=\n"
    "s_sub_u32 s72, s72, 1\n"
    "buffer_load_dwordx4 v[24:27], v3, s[16:19], s54 offen sc1\n"
    "s_waitcnt vmcnt(0)\n"
    "v_cmp_ne_u64 vcc, v[26:27], s[56:57]\n"
    "s_cbranch_vccnz .Lstr1_%=\n"
    "s_branch .Lstb2_%=\n"
    ".Lcar3_%=:\n"
    "s_sleep 1\n"
    "s_cmp_eq_u32 s72, 0\n"
    "s_cbranch_scc1 .Lcab4_%=\n"
    "s_sub_u32 s72, s72, 1\n"
    "buffer_load_dwordx4 v[34:37], v9, s[16:19], 0 offen sc1\n"
    "buffer_load_dwordx4 v[38:41], v10, s[16:19], 0 offen sc1\n"
    "buffer_load_dwordx4 v[42:45], v11, s[16:19], 0 offen sc1\n"
    "buffer_load_dwordx4 v[72:75], v28, s[16:19], 0 offen sc1\n"
    "buffer_load_dwordx4 v[78:81], v29, s[16:19], 0 offen sc1\n"
    "buffer_load_dwordx4 v[82:85], v30, s[16:19], 0 offen sc1\n"
    "buffer_load_dwordx4 v[96:99], v31, s[16:19], 0 offen sc1\n"
    "buffer_load_dwordx4 v[100:103], v32, s[16:19], 0 offen sc1\n"
    "s_waitcnt vmcnt(0)\n"
    "v_cmp_ne_u64 s[76:77], v[36:37], s[56:57]\n"
    "v_cmp_ne_u64 s[78:79], v[40:41], s[56:57]\n"
    "v_cmp_ne_u64 s[84:85], v[44:45], s[56:57]\n"
    "v_cmp_ne_u64 s[86:87], v[74:75], s[56:57]\n"
    "s_and_b64 s[76:77], s[76:77], s[8:9]\n"
    "s_and_b64 s[78:79], s[78:79], s[10:11]\n"
    "s_and_b64 s[84:85], s[84:85], s[12:13]\n"
    "s_and_b64 s[86:87], s[86:87], s[14:15]\n"
    "s_or_b64 s[76:77], s[76:77], s[78:79]\n"
    "s_or_b64 s[84:85], s[84:85], s[86:87]\n"
    "s_or_b64 s[74:75], s[76:77], s[84:85]\n"
    "v_cmp_ne_u64 s[76:77], v[80:81], s[56:57]\n"
    "v_cmp_ne_u64 s[78:79], v[84:85], s[56:57]\n"
    "v_cmp_ne_u64 s[84:85], v[98:99], s[56:57]\n"
    "v_cmp_ne_u64 s[86:87], v[102:103], s[56:57]\n"
    "s_and_b64 s[76:77], s[76:77], s[34:35]\n"
    "s_and_b64 s[78:79], s[78:79], s[40:41]\n"
    "s_and_b64 s[84:85], s[84:85], s[42:43]\n"
    "s_and_b64 s[86:87], s[86:87], s[44:45]\n"
    "s_or_b64 s[76:77], s[76:77], s[78:79]\n"
    "s_or_b64 s[84:85], s[84:85], s[86:87]\n"
    "s_or_b64 s[76:77], s[76:77], s[84:85]\n"
    "s_or_b64 vcc, s[76:77], s[74:75]\n"
    "s_cbranch_vccnz .Lcar3_%=\n"
    "s_branch .Lcab4_%=\n"
    ".Lfmr5_%=:\n"
    "s_sleep 1\n"
    "s_cmp_eq_u32 s72, 0\n"
    "s_cbranch_scc1 .Lfmb6_%=\n"
    "s_sub_u32 s72, s72, 1\n"
    "buffer_load_dwordx2 v[136:137], v69, s[16:19], s88 offen sc1\n"
    "s_waitcnt vmcnt(0)\n"
    "v_cmp_ne_u64 vcc, v[136:137], s[56:57]\n"
    "s_cbranch_vccnz .Lfmr5_%=\n"
    "s_branch .Lfmb6_%=\n"
    ".Lfsr7_%=:\n"
    "s_sleep 1\n"
    "s_cmp_eq_u32 s72, 0\n"
    "s_cbranch_scc1 .Lfsb8_%=\n"
    "s_sub_u32 s72, s72, 1\n"
    "buffer_load_dwordx2 v[136:137], v69, s[16:19], s89 offen sc1\n"
    "s_waitcnt vmcnt(0)\n"
    "v_cmp_ne_u64 vcc, v[136:137], s[56:57]\n"
    "s_cbranch_vccnz .Lfsr7_%=\n"
    "s_branch .Lfsb8_%=\n"
    ".Lxir9_%=:\n"
    "s_sleep 1\n"
    "s_cmp_eq_u32 s72, 0\n"
    "s_cbranch_scc1 .Lxib10_%=\n"
    "s_sub_u32 s72, s72, 1\n"
    "buffer_load_dwordx4 v[220:223], v3, s[16:19], s21 offen offset:0 sc1\n"
    "buffer_load_dwordx4 v[224:227], v3, s[16:19], s21 offen offset:1024 sc1\n"
    "buffer_load_dwordx4 v[228:231], v3, s[16:19], s21 offen offset:2048 sc1\n"
    "buffer_load_dwordx4 v[232:235], v3, s[16:19], s21 offen offset:3072 sc1\n"
    "s_waitcnt vmcnt(0)\n"
    "v_cmp_ne_u64 s[74:75], v[222:223], s[56:57]\n"
    "v_cmp_ne_u64 vcc, v[226:227], s[56:57]\n"
    "s_or_b64 s[74:75], s[74:75], vcc\n"
    "v_cmp_ne_u64 vcc, v[230:231], s[56:57]\n"
    "s_or_b64 s[74:75], s[74:75], vcc\n"
    "v_cmp_ne_u64 vcc, v[234:235], s[56:57]\n"
    "s_or_b64 vcc, s[74:75], vcc\n"
    "s_cbranch_vccnz .Lxir9_%=\n"
    "s_branch .Lxib10_%=\n"
    ".Lend_%=:\n"
    :
    : "s"(__builtin_amdgcn_kernarg_segment_ptr()), "s"(blockIdx.x), "v"(threadIdx.x), "s"((unsigned)(size_t)lds)
    : "v1", "v2", "v3", "v4", "v5", "v6", "v7", "v8", "v9", "v10", "v11", "v12", "v13", "v14", "v15", "v16", "v17", "v18", "v19", "v20", "v21", "v22", "v23", "v24", "v25", "v26", "v27", "v28", "v29", "v30", "v31", "v32", "v33", "v34", "v35", "v36", "v37", "v38", "v39", "v40", "v41", "v42", "v43", "v44", "v45", "v46", "v47", "v48", "v49", "v50", "v51", "v52", "v53", "v54", "v55", "v56", "v57", "v58", "v59", "v60", "v61", "v62", "v63", "v64", "v65", "v66", "v67", "v68", "v69", "v70", "v71", "v72", "v73", "v74", "v75", "v76", "v77", "v78", "v79", "v80", "v81", "v82", "v83", "v84", "v85", "v86", "v87", "v88", "v89", "v90", "v91", "v92", "v93", "v94", "v95", "v96", "v97", "v98", "v99", "v100", "v101", "v102", "v103", "v104", "v105", "v106", "v107", "v108", "v109", "v110", "v111", "v112", "v113", "v114", "v115", "v116", "v117", "v118", "v119", "v120", "v121", "v122", "v123", "v124", "v125", "v126", "v127", "v128", "v129", "v130", "v131", "v132", "v133", "v134", "v135", "v136", "v137", "v138", "v139", "v140", "v141", "v142", "v143", "v144", "v145", "v146", "v147", "v148", "v149", "v150", "v151", "v152", "v153", "v154", "v155", "v156", "v157", "v158", "v159", "v160", "v161", "v162", "v163", "v164", "v165", "v166", "v167", "v168", "v169", "v170", "v171", "v172", "v173", "v174", "v175", "v176", "v177", "v178", "v179", "v180", "v181", "v182", "v183", "v184", "v185", "v186", "v187", "v188", "v189", "v190", "v191", "v192", "v193", "v194", "v195", "v196", "v197", "v198", "v199", "v200", "v201", "v202", "v203", "v204", "v205", "v206", "v207", "v208", "v209", "v210", "v211", "v212", "v213", "v214", "v215", "v216", "v217", "v218", "v219", "v220", "v221", "v222", "v223", "v224", "v225", "v226", "v227", "v228", "v229", "v230", "v231", "v232", "v233", "v234", "v235", "v236", "v237", "v238", "v239", "v240", "v241", "v242", "v243", "v244", "v245", "v246", "v247", "v248", "v249", "v250", "v251", "v252", "v253", "v254", "v255", "a0", "a1", "a2", "a3", "a4", "a5", "a6", "a7", "a8", "a9", "a10", "a11", "a12", "a13", "a14", "a15", "a16", "a17", "a18", "a19", "a20", "a21", "a22", "a23", "a24", "a25", "a26", "a27", "a28", "a29", "a30", "a31", "a32", "a33", "a34", "a35", "a36", "a37", "a38", "a39", "a40", "a41", "a42", "a43", "a44", "a45", "a46", "a47", "a48", "a49", "a50", "a51", "a52", "a53", "a54", "a55", "a56", "a57", "a58", "a59", "a60", "a61", "a62", "a63", "a64", "a65", "a66", "a67", "a68", "a69", "a70", "a71", "a72", "a73", "a74", "a75", "a76", "a77", "a78", "a79", "a80", "a81", "a82", "a83", "a84", "a85", "a86", "a87", "a88", "a89", "a90", "a91", "a92", "a93", "a94", "a95", "a96", "a97", "a98", "a99", "a100", "a101", "a102", "a103", "a104", "a105", "a106", "a107", "a108", "a109", "a110", "a111", "a112", "a113", "a114", "a115", "a116", "a117", "a118", "a119", "a120", "a121", "a122", "a123", "a124", "a125", "a126", "a127", "a128", "a129", "a130", "a131", "a132", "a133", "a134", "a135", "a136", "a137", "a138", "a139", "a140", "a141", "a142", "a143", "a144", "a145", "a146", "a147", "a148", "a149", "a150", "a151", "a152", "a153", "a154", "a155", "a156", "a157", "a158", "a159", "a160", "a161", "a162", "a163", "a164", "a165", "a166", "a167", "a168", "a169", "a170", "a171", "a172", "a173", "a174", "a175", "a176", "a177", "a178", "a179", "a180", "a181", "a182", "a183", "a184", "a185", "a186", "a187", "a188", "a189", "a190", "a191", "a192", "a193", "a194", "a195", "a196", "a197", "a198", "a199", "a200", "a201", "a202", "a203", "a204", "a205", "a206", "a207", "a208", "a209", "a210", "a211", "a212", "a213", "a214", "a215", "a216", "a217", "a218", "a219", "a220", "a221", "a222", "a223", "a224", "a225", "a226", "a227", "a228", "a229", "a230", "a231", "a232", "a233", "a234", "a235", "a236", "a237", "a238", "a239", "a240", "a241", "a242", "a243", "a244", "a245", "a246", "a247", "a248", "a249", "a250", "a251", "a252", "a253", "a254", "a255", "s4", "s5", "s6", "s7", "s8", "s9", "s10", "s11", "s12", "s13", "s14", "s15", "s16", "s17", "s18", "s19", "s20", "s21", "s22", "s23", "s24", "s25", "s26", "s27", "s28", "s29", "s30", "s31", "s33", "s34", "s35", "s36", "s37", "s38", "s39", "s40", "s41", "s42", "s43", "s44", "s45", "s46", "s47", "s48", "s49", "s50", "s51", "s52", "s53", "s54", "s55", "s56", "s57", "s58", "s59", "s60", "s61", "s62", "s63", "s64", "s65", "s66", "s67", "s68", "s69", "s70", "s71", "s72", "s73", "s74", "s75", "s76", "s77", "s78", "s79", "s80", "s81", "s82", "s83", "s84", "s85", "s86", "s87", "s88", "s89", "s90", "s91", "s92", "s93", "s94", "s95", "s96", "s97", "s98", "s99", "vcc", "scc", "memory");
}

extern "C" void kernel_launch(void* const* d_in, const int* in_sizes, int n_in, void* d_out, int out_size, void* d_ws,
                              size_t ws_size, hipStream_t stream) {
  s5_mlp_fused<<<256, 256, 0, stream>>>(d_in[0], d_in[3], d_in[4], d_in[10], d_ws, d_in[1], d_in[2], d_in[7], d_in[11],
                                        d_in[12], d_in[5], d_in[6], d_in[8], d_in[13], d_out);
}
```
